```python
import torch

def _softmax(x, axis=-1):
    return torch.softmax(x, dim=axis)
def _gelu(x):
    return torch.nn.functional.gelu(x, approximate="tanh")
def _silu(x):
    return torch.nn.functional.silu(x)
def _sigmoid(x):
    return torch.sigmoid(x)
def _layernorm(x, axis=-1, eps=1e-5):
    m = x.mean(dim=axis, keepdim=True)
    v = x.var(dim=axis, keepdim=True, unbiased=False)
    return (x - m) / torch.sqrt(v + eps)
def _scatter_add(tgt, idx, src, axis=0):
    return tgt.clone().index_add_(axis, idx, src)
def _segment_sum(src, seg, n):
    out = torch.zeros((n,) + src.shape[1:], dtype=src.dtype)
    return out.index_add_(0, seg, src)
def _pad(x, pw):
    flat = tuple(v for pair in reversed(pw) for v in pair)
    return torch.nn.functional.pad(x, flat)

kernel_name = 'gated_mix__ssm_conv_h3__compressed_kv_cla__prompt'

def setup_inputs():

    _w = (lambda a: (lambda t: t.to(torch.bfloat16) if t.is_floating_point() else t)(torch.as_tensor(a)))
    def _A_setup():
        B, S, D = 1, 2048, 640
        ks = 4
        return {
            "q": _w((lambda *s: torch.randn(*s, dtype=torch.bfloat16) * 0.05)(B, S, D)),
            "k": _w((lambda *s: torch.randn(*s, dtype=torch.bfloat16) * 0.4)(B, S, D)),
            "v": _w((lambda *s: torch.randn(*s, dtype=torch.bfloat16) * 0.4)(B, S, D)),
            "shift_k": _w((lambda *s: torch.randn(*s, dtype=torch.bfloat16) * 0.4)(D, ks) * 0.5),
            "ssm_k": _w((lambda *s: torch.randn(*s, dtype=torch.bfloat16) * 0.8)(D, S) * (1.0 / S ** 0.5)),
            "ssm_D": _w((lambda *s: torch.randn(*s, dtype=torch.bfloat16) * 0.4)(D)),
        }
    _A_in = _A_setup()

    def _B_setup():
        B, H, Sq, Sk, D, M = 1, 32, 2048, 2048, 128, 640
        Dc = 4 * D
        return {
            "x": _w((lambda *s: torch.randn(*s, dtype=torch.bfloat16) * 0.05)(B, Sq, M)),
            "c_kv": _w((lambda *s: torch.randn(*s, dtype=torch.bfloat16) * 0.4)(B, Sk, Dc)),
            "w_q": _w((lambda *s: torch.randn(*s, dtype=torch.bfloat16) * 6.4)(M, H, D) * (M ** -0.5)),
            "w_uk": _w((lambda *s: torch.randn(*s, dtype=torch.bfloat16) * 6.4)(Dc, H, D) * (Dc ** -0.5)),
            "w_uv": _w((lambda *s: torch.randn(*s, dtype=torch.bfloat16) * 0.4)(Dc, H, D) * (Dc ** -0.5)),
            "w_o": _w((lambda *s: torch.randn(*s, dtype=torch.bfloat16) * 0.4)(H, D, M) * ((H * D) ** -0.5)),
        }
    _B_in = _B_setup()

    return {
        "A_q": _A_in["q"],
        "A_k": _A_in["k"],
        "A_v": _A_in["v"],
        "A_shift_k": _A_in["shift_k"],
        "A_ssm_k": _A_in["ssm_k"],
        "A_ssm_D": _A_in["ssm_D"],
        "B_x": _B_in["x"],
        "B_c_kv": _B_in["c_kv"],
        "B_w_q": _B_in["w_q"],
        "B_w_uk": _B_in["w_uk"],
        "B_w_uv": _B_in["w_uv"],
        "B_w_o": _B_in["w_o"],
        "gate_w": _w((lambda *s: torch.randn(*s, dtype=torch.bfloat16) * 0.05)(640) * 16.0),
    }

def reference(A_q, A_k, A_v, A_shift_k, A_ssm_k, A_ssm_D, B_x, B_c_kv, B_w_q, B_w_uk, B_w_uv, B_w_o, gate_w):
    def _A_ref(q, k, v, shift_k, ssm_k, ssm_D):
        B, S, D = q.shape
        ks = shift_k.shape[-1]
        k_pad = _pad(k, ((0, 0), (ks - 1, 0), (0, 0)))
        k_shift = torch.zeros_like(k)
        for t in range(ks):
            k_shift = k_shift + k_pad[:, t:t + S, :] * shift_k[:, ks - 1 - t][None, None, :]
        kv = k_shift * v
        L = 2 * S
        kv_pad = _pad(kv, ((0, 0), (0, S), (0, 0)))
        kf_pad = _pad(ssm_k, ((0, 0), (0, S)))
        kv_f = (lambda x, axes=(-2,-1): torch.fft.rfft2(x.float(), dim=axes))(kv_pad[:, None, :, :], axes=(1, 2))
        kf_f = (lambda x, axes=(-2,-1): torch.fft.rfft2(x.float(), dim=axes))(kf_pad[:, None, :], axes=(1, 2))
        kf_f = (lambda x, *d: x.permute(*d))(kf_f, 1, 2, 0)
        y_f = kv_f * kf_f[None, :, :, :]
        y = (lambda x, s, axes=(-2,-1): torch.fft.irfft2(x, s=s, dim=axes).to(torch.bfloat16))(y_f, (1, L), axes=(1, 2))[:, 0, :S, :]
        y = y + kv * ssm_D[None, None, :]
        return q * y

    def _B_ref(x, c_kv, w_q, w_uk, w_uv, w_o):
        B, Sq, M = x.shape[0], x.shape[1], x.shape[2]
        Sk = c_kv.shape[1]
        H, D = w_q.shape[1], w_q.shape[2]
        q = torch.einsum("bqm,mhd->bhqd", x, w_q)
        k = torch.einsum("bkc,chd->bhkd", c_kv, w_uk)
        v = torch.einsum("bkc,chd->bhkd", c_kv, w_uv)
        scores = torch.einsum("bhqd,bhkd->bhqk", q, k) * (D ** -0.5)
        off = Sk - Sq
        qi = torch.arange(Sq)[:, None]
        kj = torch.arange(Sk)[None, :]
        cmask = kj <= (qi + off)
        scores = torch.where(cmask[None, None], scores, -1e9)
        p = _softmax(scores, axis=-1)
        ctx = torch.einsum("bhqk,bhkd->bhqd", p, v)
        return torch.einsum("bhqd,hdm->bqm", ctx, w_o)

    x = A_q
    g = _sigmoid((x * gate_w).sum(-1, keepdims=True))
    a = _A_ref(x, A_k, A_v, A_shift_k, A_ssm_k, A_ssm_D)
    b = _B_ref(x, B_c_kv, B_w_q, B_w_uk, B_w_uv, B_w_o)
    return g * a + (1 - g) * b
```

```cpp
#include <hip/hip_runtime.h>
#include <hip/hip_bf16.h>
#include <stdint.h>

#define DI __device__ __forceinline__
#define LAS __attribute__((address_space(3)))
typedef __bf16 bf16x8 __attribute__((ext_vector_type(8)));
typedef __bf16 bf16x4 __attribute__((ext_vector_type(4)));
typedef __bf16 bf16x2 __attribute__((ext_vector_type(2)));
typedef float f32x16 __attribute__((ext_vector_type(16)));
typedef float f32x4 __attribute__((ext_vector_type(4)));
typedef float f32x2 __attribute__((ext_vector_type(2)));
typedef unsigned u32x4 __attribute__((ext_vector_type(4)));
typedef unsigned u32x2 __attribute__((ext_vector_type(2)));
typedef unsigned short u16;

constexpr int S = 2048, DM = 640, NH = 32, HD = 128, DC = 512, NQ = 4096;
constexpr size_t MB = 1u << 20;
constexpr size_t WS_K = 0, WS_V = 16 * MB, WS_Q = 32 * MB, WS_CTX = 48 * MB, WS_WO = 64 * MB, WS_UT = 72 * MB,
                 WS_XT = 76 * MB, WS_GA = 80 * MB, WS_G = 84 * MB;
#define SRD3 0x00020000

DI unsigned rfl(unsigned x) { return __builtin_amdgcn_readfirstlane(x); }
DI unsigned cvtpk(float lo, float hi) { f32x2 v = {lo, hi}; bf16x2 b = __builtin_convertvector(v, bf16x2); return __builtin_bit_cast(unsigned, b); }
DI float rbf(float x) { return (float)(__bf16)x; }
DI float bf2f(u16 v) { return __uint_as_float((unsigned)v << 16); }
DI float lo16(unsigned v) { return __uint_as_float(v << 16); }
DI float hi16(unsigned v) { return __uint_as_float(v & 0xffff0000u); }
DI f32x16 mfma32(bf16x8 a, bf16x8 b, f32x16 c) { return __builtin_amdgcn_mfma_f32_32x32x16_bf16(a, b, c, 0, 0, 0); }
DI f32x4 mfma16(bf16x8 a, bf16x8 b, f32x4 c) { return __builtin_amdgcn_mfma_f32_16x16x32_bf16(a, b, c, 0, 0, 0); }
DI bf16x8 lds_rd128(unsigned addr) { return *(const LAS bf16x8*)(size_t)addr; }
DI bf16x8 lds_rdtr(unsigned addr) {
  bf16x4 lo = __builtin_amdgcn_ds_read_tr16_b64_v4bf16((LAS bf16x4*)(size_t)addr);
  bf16x4 hi = __builtin_amdgcn_ds_read_tr16_b64_v4bf16((LAS bf16x4*)(size_t)(addr + 256));
  return __builtin_shufflevector(lo, hi, 0, 1, 2, 3, 4, 5, 6, 7);
}
#define DMA16(m0v, voff, srd, soff) \
  asm volatile("s_mov_b32 m0, %0\n\ts_nop 0\n\tbuffer_load_dwordx4 %1, %2, %3 offen lds" ::"s"(m0v), "v"(voff), "s"(srd), "s"(soff) : "memory")
#define DMA16_OFF(voff, srd, soff, imm) \
  asm volatile("buffer_load_dwordx4 %0, %1, %2 offen offset:%3 lds" ::"v"(voff), "s"(srd), "s"(soff), "i"(imm) : "memory")
#define SET_M0(m0v) asm volatile("s_mov_b32 m0, %0\n\ts_nop 0" ::"s"(m0v) : "memory")
#define SBAR() __builtin_amdgcn_sched_barrier(0)
DI u32x4 pack8(const f32x16& a, int s) {
  u32x4 w;
  w[0] = cvtpk(a[8 * s + 0], a[8 * s + 1]); w[1] = cvtpk(a[8 * s + 2], a[8 * s + 3]);
  w[2] = cvtpk(a[8 * s + 4], a[8 * s + 5]); w[3] = cvtpk(a[8 * s + 6], a[8 * s + 7]);
  return w;
}

__global__ __launch_bounds__(256) void prepass_kernel(const u16* __restrict__ x, const u16* __restrict__ k, const u16* __restrict__ v,
                                                       const u16* __restrict__ shk, const u16* __restrict__ gw, u16* __restrict__ uT,
                                                       u16* __restrict__ xT, float* __restrict__ gbuf) {
  __shared__ u16 su[80 * 66], sx[80 * 66];
  const int b = blockIdx.x, tid = threadIdx.x;
  const int t0 = (b >> 3) * 64, c0 = (b & 7) * 80;
  for (int it = tid; it < 640; it += 256) {
    const int cc = it % 10, tl = it / 10, t = t0 + tl, c = c0 + cc * 8;
    u32x4 kr[4];
#pragma unroll
    for (int j = 0; j < 4; ++j) {
      const int tt = t - j;
      kr[j] = tt >= 0 ? *(const u32x4*)(k + (size_t)tt * DM + c) : (u32x4){0, 0, 0, 0};
    }
    const u32x4 vr = *(const u32x4*)(v + (size_t)t * DM + c);
    const u32x4 xr = *(const u32x4*)(x + (size_t)t * DM + c);
#pragma unroll
    for (int e = 0; e < 8; ++e) {
      const u32x2 w = *(const u32x2*)(shk + (size_t)(c + e) * 4);
      const float w0 = lo16(w[0]), w1 = hi16(w[0]), w2 = lo16(w[1]), w3 = hi16(w[1]);
      float kk[4];
#pragma unroll
      for (int j = 0; j < 4; ++j) kk[j] = (e & 1) ? hi16(kr[j][e >> 1]) : lo16(kr[j][e >> 1]);
      float a = rbf(kk[3] * w3);
      a = rbf(a + rbf(kk[2] * w2));
      a = rbf(a + rbf(kk[1] * w1));
      a = rbf(a + rbf(kk[0] * w0));
      const float vv = (e & 1) ? hi16(vr[e >> 1]) : lo16(vr[e >> 1]);
      const float kv = rbf(a * vv);
      su[(cc * 8 + e) * 66 + tl] = (u16)(__float_as_uint(kv) >> 16);
      sx[(cc * 8 + e) * 66 + tl] = (u16)((e & 1) ? (xr[e >> 1] >> 16) : (xr[e >> 1] & 0xffffu));
    }
  }
  __syncthreads();
  for (int it = tid; it < 640; it += 256) {
    const int tc = it & 7, cl = it >> 3;
    u32x4 wu, wx;
#pragma unroll
    for (int e = 0; e < 4; ++e) {
      wu[e] = (unsigned)su[cl * 66 + tc * 8 + 7 - 2 * e] | ((unsigned)su[cl * 66 + tc * 8 + 6 - 2 * e] << 16);
      wx[e] = (unsigned)sx[cl * 66 + tc * 8 + 2 * e] | ((unsigned)sx[cl * 66 + tc * 8 + 2 * e + 1] << 16);
    }
    *(u32x4*)(uT + (size_t)(c0 + cl) * S + t0 + tc * 8) = wu;
    *(u32x4*)(xT + (size_t)(c0 + cl) * S + t0 + tc * 8) = wx;
  }
  const int wid = tid >> 6, lane = tid & 63;
  for (int rr = 0; rr < 2; ++rr) {
    const int t = b * 8 + wid * 2 + rr;
    float s = 0.f;
#pragma unroll
    for (int i = 0; i < 10; ++i) { const int c = lane + 64 * i; s += rbf(bf2f(x[(size_t)t * DM + c]) * bf2f(gw[c])); }
#pragma unroll
    for (int o = 32; o >= 1; o >>= 1) s += __shfl_xor(s, o);
    if (lane == 0) {
      const float sb = rbf(s);
      const float g = rbf(1.f / (1.f + __expf(-sb)));
      gbuf[t] = g; gbuf[S + t] = rbf(1.f - g);
    }
  }
}

__global__ __launch_bounds__(256) void wo_frag_kernel(const u16* __restrict__ wo, u16* __restrict__ wf) {
  const int id = blockIdx.x * 256 + threadIdx.x;
  const int r = id & 15, g = (id >> 4) & 3, ks = (id >> 6) & 127, nt = id >> 13;
  const int kb = ks * 32 + 16 * (g >> 1) + 4 * (g & 1);
  u32x4 w;
#pragma unroll
  for (int e = 0; e < 4; ++e) {
    const int j0 = 2 * e, j1 = 2 * e + 1;
    const int k0 = kb + 8 * (j0 >> 2) + (j0 & 3), k1 = kb + 8 * (j1 >> 2) + (j1 & 3);
    w[e] = (unsigned)wo[(size_t)k0 * DM + nt * 16 + r] | ((unsigned)wo[(size_t)k1 * DM + nt * 16 + r] << 16);
  }
  *(u32x4*)(wf + (size_t)id * 8) = w;
}

struct PFrag { bf16x8 x[4]; bf16x8 wk, wv; };
struct PCtx {
  unsigned xa[4];
  unsigned wa;
  unsigned vxkv[2], vxq[2], vw;
  unsigned m0x, m0w;
  unsigned sxkv, sxq, swo;
  __amdgpu_buffer_rsrc_t rc, rx, rwk, rwv, rwq;
};
template <int STG, int KS, bool KV> DI void p_read(PFrag& f, const PCtx& c) {
#pragma unroll
  for (int i = 0; i < 4; ++i) f.x[i] = lds_rd128(c.xa[KS] + STG * 32768 + i * 4096);
  f.wk = lds_rdtr(c.wa + STG * 32768 + KS * 8192);
  if constexpr (KV) f.wv = lds_rdtr(c.wa + STG * 32768 + KS * 8192 + 2048);
}
template <bool KV> DI void p_mma(const PFrag& f, f32x16 (&aK)[4], f32x16 (&aV)[4]) {
#pragma unroll
  for (int i = 0; i < 4; ++i) {
    aK[i] = mfma32(f.wk, f.x[i], aK[i]);
    if constexpr (KV) aV[i] = mfma32(f.x[i], f.wv, aV[i]);
  }
}
template <int STG, int DK> DI void p_dma(const PCtx& c, int kstep) {
  if constexpr (DK == 1) {
#pragma unroll
    for (int j = 0; j < 4; ++j) DMA16(c.m0x + STG * 32768 + j * 1024, c.vxkv[j & 1], c.rc, c.sxkv + j * 8192 + kstep * 128);
#pragma unroll
    for (int j = 0; j < 4; ++j) {
      if (j < 2) DMA16(c.m0w + STG * 32768 + j * 1024, c.vw, c.rwk, c.swo + (j & 1) * 128 + kstep * (64 * 8192));
      else       DMA16(c.m0w + STG * 32768 + j * 1024, c.vw, c.rwv, c.swo + (j & 1) * 128 + kstep * (64 * 8192));
    }
  } else if constexpr (DK == 2) {
#pragma unroll
    for (int j = 0; j < 4; ++j) DMA16(c.m0x + STG * 32768 + j * 1024, c.vxq[j & 1], c.rx, c.sxq + j * (8 * 1280) + kstep * 128);
#pragma unroll
    for (int j = 0; j < 2; ++j) DMA16(c.m0w + STG * 32768 + j * 1024, c.vw, c.rwq, c.swo + j * 128 + kstep * (64 * 8192));
  }
}
template <int STG, bool KV, int DK, int NEXT, int VM> DI void p_step(PFrag& f0, PFrag& f1, f32x16 (&aK)[4], f32x16 (&aV)[4], const PCtx& c, int dk) {
  p_read<STG, 1, KV>(f1, c); p_mma<KV>(f0, aK, aV);
  p_read<STG, 2, KV>(f0, c); p_mma<KV>(f1, aK, aV);
  p_read<STG, 3, KV>(f1, c); p_mma<KV>(f0, aK, aV);
  SBAR();
  if constexpr (VM == 0) asm volatile("s_waitcnt vmcnt(0) lgkmcnt(0)\n\ts_barrier" ::: "memory");
  else                   asm volatile("s_waitcnt vmcnt(16) lgkmcnt(0)\n\ts_barrier" ::: "memory");
  SBAR();
  p_dma<STG, DK>(c, dk);
  if constexpr (NEXT == 1) p_read<STG ^ 1, 0, true>(f0, c);
  if constexpr (NEXT == 2) p_read<STG ^ 1, 0, false>(f0, c);
  p_mma<KV>(f1, aK, aV);
}

__global__ __launch_bounds__(512, 2) void proj_kernel(const u16* __restrict__ x, const u16* __restrict__ ckv, const u16* __restrict__ wq,
                                                       const u16* __restrict__ wuk, const u16* __restrict__ wuv, u16* __restrict__ Qg,
                                                       u16* __restrict__ Kg, u16* __restrict__ Vg) {
  extern __shared__ __attribute__((aligned(16))) char lds[];
  const unsigned tid = threadIdx.x, lane = tid & 63u, w = rfl(tid >> 6), r = lane & 31u, hi = lane >> 5;
  const unsigned bid = blockIdx.x, idx = bid >> 3, h = (bid & 7u) * 4u + (idx >> 3), p = idx & 7u;
  const unsigned wm = w >> 2, wn = w & 3u;
  const unsigned ldsb = (unsigned)(size_t)(LAS char*)lds;
  PCtx c;
  {
    const unsigned bx = ldsb + (128u * wm + r) * 128u + ((hi ^ ((r >> 1) & 7u)) << 4);
#pragma unroll
    for (int ks = 0; ks < 4; ++ks) c.xa[ks] = bx ^ (unsigned)(ks << 5);
    c.wa = ldsb + 65536u + hi * 4096u + ((lane >> 2) & 3u) * 64u + ((lane >> 4) & 1u) * 32u + (lane & 3u) * 8u + wn * 512u;
#pragma unroll
    for (int par = 0; par < 2; ++par) {
      const unsigned ch = ((lane & 7u) ^ ((4u * par + (lane >> 4)) & 7u)) * 16u;
      c.vxkv[par] = (lane >> 3) * 1024u + ch;
      c.vxq[par] = (lane >> 3) * 1280u + ch;
    }
    c.vw = ((lane >> 2) & 7u) * 8192u + (lane >> 5) * 64u + (lane & 3u) * 16u;
    c.m0x = ldsb + w * 4096u; c.m0w = ldsb + 65536u + w * 4096u;
    c.sxkv = (256u * p + 32u * w) * 1024u;
    c.sxq = ((w < 4 ? 128u * p + 32u * w : 128u * (15u - p) + 32u * (w - 4u))) * 1280u;
    c.swo = (8u * w) * 8192u + h * 256u;
    c.rc = __builtin_amdgcn_make_buffer_rsrc((void*)ckv, (short)0, S * DC * 2, SRD3);
    c.rx = __builtin_amdgcn_make_buffer_rsrc((void*)x, (short)0, S * DM * 2, SRD3);
    c.rwk = __builtin_amdgcn_make_buffer_rsrc((void*)wuk, (short)0, DC * NQ * 2, SRD3);
    c.rwv = __builtin_amdgcn_make_buffer_rsrc((void*)wuv, (short)0, DC * NQ * 2, SRD3);
    c.rwq = __builtin_amdgcn_make_buffer_rsrc((void*)wq, (short)0, DM * NQ * 2, SRD3);
  }
  f32x16 aK[4], aV[4];
#pragma unroll
  for (int i = 0; i < 4; ++i) { aK[i] = f32x16{}; aV[i] = f32x16{}; }
  PFrag f0, f1;
  p_dma<0, 1>(c, 0); p_dma<1, 1>(c, 1);
  asm volatile("s_waitcnt vmcnt(8)\n\ts_barrier" ::: "memory");
  SBAR();
  p_read<0, 0, true>(f0, c);
  for (int u = 0; u < 6; u += 2) {
    p_step<0, true, 1, 1, 0>(f0, f1, aK, aV, c, u + 2);
    p_step<1, true, 1, 1, 0>(f0, f1, aK, aV, c, u + 3);
  }
  p_step<0, true, 2, 1, 0>(f0, f1, aK, aV, c, 0);
  p_step<1, true, 2, 2, 0>(f0, f1, aK, aV, c, 1);
  {
#pragma unroll
    for (int i = 0; i < 4; ++i) {
      const unsigned kt = 8u * p + 4u * wm + i;
#pragma unroll
      for (int s = 0; s < 2; ++s) {
        *(u32x4*)(Kg + ((size_t)((h * 64u + kt) * 8u + 2u * wn + s) * 64u + lane) * 8u) = pack8(aK[i], s);
        *(u32x4*)(Vg + ((size_t)(((h * 64u + kt) * 2u + s) * 4u + wn) * 64u + lane) * 8u) = pack8(aV[i], s);
      }
      aK[i] = f32x16{};
    }
  }
  SBAR();
  p_step<0, false, 2, 2, 16>(f0, f1, aK, aV, c, 2);
  p_step<1, false, 2, 2, 0>(f0, f1, aK, aV, c, 3);
  for (int u = 2; u < 8; u += 2) {
    p_step<0, false, 2, 2, 0>(f0, f1, aK, aV, c, u + 2);
    p_step<1, false, 2, 2, 0>(f0, f1, aK, aV, c, u + 3);
  }
  p_step<0, false, 0, 2, 0>(f0, f1, aK, aV, c, 0);
  p_step<1, false, 0, 0, 0>(f0, f1, aK, aV, c, 0);
  {
    const float sc = 0.08838834764831845f * 1.4426950408889634f;
#pragma unroll
    for (int i = 0; i < 4; ++i) {
      const unsigned qt = (wm == 0 ? 4u * p : 4u * (15u - p)) + i;
      f32x16 t = aK[i] * sc;
#pragma unroll
      for (int s = 0; s < 2; ++s) *(u32x4*)(Qg + ((size_t)((h * 64u + qt) * 8u + 2u * wn + s) * 64u + lane) * 8u) = pack8(t, s);
    }
  }
}

constexpr float THR = 8.f;
#define PINV(x) asm volatile("" : "+v"(x))
struct AState {
  f32x16 O[4];
  bf16x8 Q[8];
  f32x16 S1, negm;
  unsigned P[4][4];
  bf16x8 kf[4];
  float mhat, l0, l1, fsave, mA, lA;
  unsigned kaddr, vaddr;
};
struct AUni {
  unsigned vlin;
  unsigned m0k, m0v;
  unsigned sk;
  unsigned ldsb, w, h;
  int n0;
  __amdgpu_buffer_rsrc_t rk, rv, rq;
  u16* ctx;
};
DI float max3f(float a, float b, float c) { return __builtin_fmaxf(__builtin_fmaxf(a, b), c); }
DI int a_pair(const AUni& un, int u) { u = u > 16 ? 16 : u; return u < un.n0 ? u : u - un.n0; }
DI void a_dma_k(const AUni& st, int s, int slot) {
  SET_M0(st.m0k + (unsigned)slot * 32768u);
  const unsigned so = st.sk + (unsigned)s * 32768u;
  DMA16_OFF(st.vlin, st.rk, so, 0); DMA16_OFF(st.vlin, st.rk, so, 1024); DMA16_OFF(st.vlin, st.rk, so, 2048); DMA16_OFF(st.vlin, st.rk, so, 3072);
}
DI void a_dma_v(const AUni& st, int s, int slot) {
  SET_M0(st.m0v + (unsigned)slot * 32768u);
  const unsigned so = st.sk + (unsigned)s * 32768u;
  DMA16_OFF(st.vlin, st.rv, so, 0); DMA16_OFF(st.vlin, st.rv, so, 1024); DMA16_OFF(st.vlin, st.rv, so, 2048); DMA16_OFF(st.vlin, st.rv, so, 3072);
}
DI bf16x8 pfrag(const unsigned (&p)[4]) { u32x4 w = {p[0], p[1], p[2], p[3]}; return __builtin_bit_cast(bf16x8, w); }
DI void mask16(f32x16& s, int dq, int add) {
  const float ninf = -__builtin_inff();
#pragma unroll
  for (int e = 0; e < 16; ++e) {
    const int cr = (e & 3) + 8 * (e >> 2) + add;
    float x = s[e];
    asm volatile("v_cmp_gt_i32 vcc, %2, %1\n\ts_nop 1\n\tv_cndmask_b32 %0, %0, %3, vcc" : "+v"(x) : "v"(dq), "i"(cr), "v"(ninf) : "vcc");
    s[e] = x;
  }
}
DI void sub16(f32x16& s, float d) {
#pragma unroll
  for (int e = 0; e < 16; ++e) { float x = s[e]; asm volatile("v_sub_f32 %0, %0, %1" : "+v"(x) : "v"(d)); s[e] = x; }
}
DI void bcast16(f32x16& s, float d) {
#pragma unroll
  for (int e = 0; e < 16; ++e) { float x = s[e]; asm volatile("v_mov_b32 %0, %1" : "+v"(x) : "v"(d)); s[e] = x; }
}
DI void mul16(f32x16& s, float d) {
#pragma unroll
  for (int e = 0; e < 16; ++e) { float x = s[e]; asm volatile("v_mul_f32 %0, %0, %1" : "+v"(x) : "v"(d)); s[e] = x; }
}
#define EXP_PAIR(SV, i, DST) do { const float e0_ = __builtin_amdgcn_exp2f(SV[2 * (i)]), e1_ = __builtin_amdgcn_exp2f(SV[2 * (i) + 1]); \
    st.l0 += e0_; st.l1 += e1_; unsigned pk_ = cvtpk(e0_, e1_); PINV(pk_); PINV(st.l0); PINV(st.l1); DST = pk_; } while (0)
#define WG_BAR() do { asm volatile("s_waitcnt lgkmcnt(0)\n\ts_barrier" ::: "memory"); SBAR(); } while (0)

DI void a_epilogue(AState& st, const AUni& un, const float m, const float lsum, const int blk, const unsigned xa, const unsigned xb) {
  unsigned lane = threadIdx.x & 63u; PINV(lane);
  const unsigned r = lane & 31u, hi = lane >> 5, w = un.w, g = w >> 2, rw = w & 3u;
  auto pr = __builtin_amdgcn_permlane32_swap(__float_as_uint(lsum), __float_as_uint(lsum), false, false);
  const float lt = __uint_as_float(pr[0]) + __uint_as_float(pr[1]);
  LAS float* ml = (LAS float*)(size_t)xb;
  ml[w * 64 + lane] = hi ? lt : m;
  WG_BAR();
  const unsigned wo = w ^ 4u;
  const float mo = ml[wo * 64 + r], lo = ml[wo * 64 + 32 + r];
  const float ms = __builtin_fmaxf(m, mo);
  const float ws_ = __builtin_amdgcn_exp2f(m - ms), wo_ = __builtin_amdgcn_exp2f(mo - ms);
  const float sc = ws_ / (lt * ws_ + lo * wo_);
  WG_BAR();
  LAS f32x4* xs = (LAS f32x4*)(size_t)((g ? xb : xa) + rw * 8192u + lane * 16u);
  LAS f32x4* xr = (LAS f32x4*)(size_t)((g ? xa : xb) + rw * 8192u + lane * 16u);
  if (g == 0) {
#pragma unroll
    for (int q4 = 0; q4 < 8; ++q4) { f32x4 t;
#pragma unroll
      for (int e = 0; e < 4; ++e) t[e] = st.O[2 + (q4 >> 2)][4 * (q4 & 3) + e] * sc;
      xs[q4 * 64] = t; }
  } else {
#pragma unroll
    for (int q4 = 0; q4 < 8; ++q4) { f32x4 t;
#pragma unroll
      for (int e = 0; e < 4; ++e) t[e] = st.O[q4 >> 2][4 * (q4 & 3) + e] * sc;
      xs[q4 * 64] = t; }
  }
  WG_BAR();
  const unsigned qrow = (unsigned)blk * 128u + rw * 32u + r;
  u16* cp = un.ctx + ((size_t)(((qrow >> 4) * 128u + un.h * 4u + 2u * g) * 4u + hi) * 16u + (qrow & 15u)) * 8u;
#pragma unroll
  for (int dd = 0; dd < 2; ++dd) {
    f32x16 o;
#pragma unroll
    for (int q4 = 0; q4 < 4; ++q4) {
      const f32x4 t = xr[(dd * 4 + q4) * 64];
#pragma unroll
      for (int e = 0; e < 4; ++e) o[4 * q4 + e] = (g == 0 ? st.O[dd][4 * q4 + e] : st.O[2 + dd][4 * q4 + e]) * sc + t[e];
    }
#pragma unroll
    for (int s2 = 0; s2 < 2; ++s2) *(u32x4*)(cp + (dd * 4 + 2 * s2) * 128) = pack8(o, s2);
  }
}

template <bool FIRST> DI void a_step(AState& st, const AUni& un, const int u, const int dq_in) {
  bf16x8 kf[16], vf[16];
#pragma unroll
  for (int i = 0; i < 4; ++i) kf[i] = st.kf[i];
  f32x16 S0n, S1n;
  float mx0 = 0.f, mx1 = 0.f;
  int dq = dq_in;
  const bool domask = !FIRST && (u == un.n0 - 1 || u == 16);
  const bool first = FIRST || u == un.n0;
#pragma unroll
  for (int i = 0; i < 16; ++i) {
    if (i == 12) {
      asm volatile("s_waitcnt vmcnt(0) lgkmcnt(0)\n\ts_barrier" ::: "memory");
      SBAR();
    }
    if (i < 8) S0n = mfma32(kf[i], st.Q[i], i == 0 ? st.negm : S0n);
    else       S1n = mfma32(kf[i], st.Q[i - 8], i == 8 ? st.negm : S1n);
    if (i + 4 < 16) kf[i + 4] = lds_rd128(st.kaddr + (i + 4) * 1024);
    if (!FIRST && i >= 12) vf[i - 12] = lds_rd128(st.vaddr + (i - 12) * 1024);
    if (i == 12) a_dma_k(un, a_pair(un, u + 2), u & 1);
    if (i == 14) a_dma_v(un, a_pair(un, u), u & 1);
    if (!FIRST && i < 8) EXP_PAIR(st.S1, i, st.P[2 + (i >> 2)][i & 3]);
    if (!FIRST && i == 9) { if (domask) { asm volatile("s_nop 7"); PINV(dq); mask16(S0n, dq, 0); } }
    if (i >= 10 && i < 14) {
      const int b = 4 * (i - 10);
      if (i == 10) { mx0 = max3f(S0n[0], S0n[1], S0n[2]); mx1 = S0n[3]; }
      else { mx0 = max3f(mx0, S0n[b], S0n[b + 1]); mx1 = max3f(mx1, S0n[b + 2], S0n[b + 3]); }
      PINV(mx0); PINV(mx1);
    }
    SBAR();
  }
  unsigned Pn[2][4];
  bool pend = false;
#pragma unroll
  for (int j = 0; j < 16; ++j) {
    if (!FIRST) st.O[j & 3] = mfma32(vf[j], pfrag(st.P[j >> 2]), st.O[j & 3]);
    if (!FIRST && j + 4 < 16) vf[j + 4] = lds_rd128(st.vaddr + (j + 4) * 1024);
    if (j >= 12) st.kf[j - 12] = lds_rd128((st.kaddr ^ 32768u) + (j - 12) * 1024);
    if (!FIRST && j == 1) { if (domask) { asm volatile("s_nop 7"); PINV(dq); mask16(S1n, dq, 32); } }
    if (j == 2 || j == 3) {
      const int b = 8 * (j - 2);
      mx0 = max3f(mx0, S1n[b], S1n[b + 1]); mx1 = max3f(mx1, S1n[b + 2], S1n[b + 3]);
      mx0 = max3f(mx0, S1n[b + 4], S1n[b + 5]); mx1 = max3f(mx1, S1n[b + 6], S1n[b + 7]);
      PINV(mx0); PINV(mx1);
    }
    if (j == 4) {
      float mm = __builtin_fmaxf(mx0, mx1);
      auto pr = __builtin_amdgcn_permlane32_swap(__float_as_uint(mm), __float_as_uint(mm), false, false);
      const float rmax = __builtin_fmaxf(__uint_as_float(pr[0]), __uint_as_float(pr[1]));
      if (FIRST) {
        const float dl = __builtin_fmaxf(rmax, -1e30f);
        st.mhat = dl;
        sub16(S0n, dl); sub16(S1n, dl); bcast16(st.negm, -dl);
      } else if (__builtin_expect(first || __any(rmax > THR), 0)) {
        const float dl = __builtin_fmaxf(rmax, first ? -1e30f : 0.f);
        st.mhat += dl;
        if (first) { st.lA = st.l0 + st.l1; st.l0 = 0.f; st.l1 = 0.f; }
        else { st.fsave = __builtin_amdgcn_exp2f(-dl); st.l0 *= st.fsave; st.l1 *= st.fsave; pend = true; }
        sub16(S0n, dl); sub16(S1n, dl); bcast16(st.negm, -st.mhat);
      }
    }
    if (j >= 6 && j < 14) EXP_PAIR(S0n, j - 6, Pn[(j - 6) >> 2][(j - 6) & 3]);
    if (!FIRST && j == 14) {
      if (u == un.n0 - 1) {
        const unsigned qo = (un.h * 64u + (unsigned)(16 - un.n0) * 4u + (un.w & 3u)) * 8192u;
#define QLD(i) asm volatile("buffer_load_dwordx4 %0, %1, %2, %3 offen offset:%4" : "+v"(st.Q[i]) : "v"(un.vlin), "s"(un.rq), "s"(qo + ((i) >> 2) * 4096u), "i"(((i) & 3) * 1024) : "memory")
        QLD(0); QLD(1); QLD(2); QLD(3); QLD(4); QLD(5); QLD(6); QLD(7);
#undef QLD
        st.mA = st.mhat; st.mhat = 0.f; bcast16(st.negm, 0.f);
      }
    }
    SBAR();
  }
  if (!FIRST) {
    if (__builtin_expect(pend, 0)) {
      asm volatile("s_nop 7\n\ts_nop 7");
#pragma unroll
      for (int d = 0; d < 4; ++d) mul16(st.O[d], st.fsave);
    }
    if (u == un.n0 - 1) {
      asm volatile("s_waitcnt vmcnt(0)" : "+v"(st.Q[0]), "+v"(st.Q[1]), "+v"(st.Q[2]), "+v"(st.Q[3]), "+v"(st.Q[4]), "+v"(st.Q[5]), "+v"(st.Q[6]), "+v"(st.Q[7]) :: "memory");
    }
    if (u == un.n0) {
      a_epilogue(st, un, st.mA, st.lA, un.n0 - 1, un.ldsb + 65536u + (unsigned)((u - 1) & 1) * 32768u, un.ldsb + 131072u);
#pragma unroll
      for (int d = 0; d < 4; ++d) bcast16(st.O[d], 0.f);
    }
  }
#pragma unroll
  for (int e = 0; e < 4; ++e) { st.P[0][e] = Pn[0][e]; st.P[1][e] = Pn[1][e]; }
  st.S1 = S1n;
  st.kaddr ^= 32768u; st.vaddr ^= 32768u;
  SBAR();
}

__global__ __launch_bounds__(512, 2) void attn_kernel(const u16* __restrict__ Qg, const u16* __restrict__ Kg, const u16* __restrict__ Vg,
                                                       u16* __restrict__ ctx) {
  extern __shared__ __attribute__((aligned(16))) char lds[];
  const unsigned tid = threadIdx.x, lane = tid & 63u, w = rfl(tid >> 6), r = lane & 31u, hi = lane >> 5;
  const unsigned bid = blockIdx.x, idx = bid >> 3, h = (bid & 7u) * 4u + (idx >> 3), p = idx & 7u;
  const unsigned g = w >> 2, rw = w & 3u;
  const unsigned ldsb = (unsigned)(size_t)(LAS char*)lds;
  AState st;
  AUni un;
  un.vlin = lane * 16u;
  un.m0k = ldsb + w * 4096u; un.m0v = ldsb + 65536u + w * 4096u;
  un.sk = h * (64u * 8192u) + w * 4096u;
  un.ldsb = ldsb; un.w = w; un.h = h; un.n0 = 16 - (int)p; un.ctx = ctx;
  un.rk = __builtin_amdgcn_make_buffer_rsrc((void*)Kg, (short)0, NH * S * HD * 2, SRD3);
  un.rv = __builtin_amdgcn_make_buffer_rsrc((void*)Vg, (short)0, NH * S * HD * 2, SRD3);
  un.rq = __builtin_amdgcn_make_buffer_rsrc((void*)Qg, (short)0, NH * S * HD * 2, SRD3);
  st.kaddr = ldsb + g * 16384u + lane * 16u;
  st.vaddr = ldsb + 65536u + g * 16384u + lane * 16u + 32768u;
  a_dma_k(un, 0, 0);
  {
    const u16* qp = Qg + ((size_t)(h * 64u + (15u - p) * 4u + rw) * 8u * 64u + lane) * 8u;
#pragma unroll
    for (int d0 = 0; d0 < 8; ++d0) st.Q[d0] = *(const bf16x8*)(qp + d0 * 512);
  }
  a_dma_k(un, 1, 1);
#pragma unroll
  for (int d = 0; d < 4; ++d) st.O[d] = f32x16{};
  st.negm = f32x16{}; st.l0 = 0.f; st.l1 = 0.f; st.mhat = 0.f; st.fsave = 1.f; st.mA = 0.f; st.lA = 0.f;
  asm volatile("s_waitcnt vmcnt(0)\n\ts_barrier" ::: "memory");
  SBAR();
#pragma unroll
  for (int i = 0; i < 4; ++i) st.kf[i] = lds_rd128(st.kaddr + i * 1024);
  const int dq = (int)(rw * 32u + r) - (int)(g * 64u) - 4 * (int)hi;
  a_step<true>(st, un, 0, dq);
  for (int u = 1; u <= 16; ++u) a_step<false>(st, un, u, dq);
#pragma unroll
  for (int i = 0; i < 8; ++i) EXP_PAIR(st.S1, i, st.P[2 + (i >> 2)][i & 3]);
  SBAR();
  asm volatile("s_waitcnt vmcnt(0) lgkmcnt(0)\n\ts_barrier" ::: "memory");
  SBAR();
#pragma unroll
  for (int j = 0; j < 16; ++j) {
    const bf16x8 vf = lds_rd128(st.vaddr + j * 1024);
    st.O[j & 3] = mfma32(vf, pfrag(st.P[j >> 2]), st.O[j & 3]);
  }
  a_epilogue(st, un, st.mhat, st.l0 + st.l1, (int)p, ldsb, ldsb + 131072u);
}

constexpr int CV_HS = 4352 + 64;
constexpr int CV_U = 4 * CV_HS;
constexpr int CV_Y = CV_U + 96 * 80;
constexpr int CV_WAVE = CV_Y + 4096;
__global__ __launch_bounds__(256) void conv_kernel(const u16* __restrict__ hk, const u16* __restrict__ sD, const u16* __restrict__ uT,
                                                    const u16* __restrict__ xT, const float* __restrict__ gbuf, u16* __restrict__ gaT) {
  extern __shared__ __attribute__((aligned(16))) char lds[];
  const unsigned tid = threadIdx.x, lane = tid & 63u, w = rfl(tid >> 6), r = lane & 31u, hi = lane >> 5;
  const unsigned ch = blockIdx.x * 4u + w;
  char* base = lds + w * CV_WAVE;
  const u16* hrow = hk + (size_t)ch * S;
#pragma unroll
  for (int m = 0; m < 4; ++m) {
    const int sh = m == 0 ? -28 : m - 32;
    for (int i = (int)lane * 8; i < 2088; i += 512) {
      u32x4 wv;
#pragma unroll
      for (int e = 0; e < 4; ++e) {
        const int a = i + sh + 2 * e, b = a + 1;
        const unsigned lo = (a >= 0 && a < S) ? hrow[a] : 0u, hh = (b >= 0 && b < S) ? hrow[b] : 0u;
        wv[e] = lo | (hh << 16);
      }
      *(u32x4*)(base + m * CV_HS + i * 2) = wv;
    }
  }
  for (int it = lane; it < 96 * 4; it += 64) {
    const int row = it >> 2, cch = it & 3;
    u32x4 wv = {0, 0, 0, 0};
    if (row >= 32) wv = *(const u32x4*)(uT + (size_t)ch * S + (row - 32) * 32 + cch * 8);
    *(u32x4*)(base + CV_U + row * 80 + cch * 16) = wv;
  }
  __syncthreads();
  const unsigned lb = (unsigned)(size_t)(LAS char*)base;
  const int m = (r + 1) & 3;
  const int pos0 = (int)r - 8 * (int)hi - 7 + (m == 0 ? 28 : 32 - m);
  const unsigned aaddr = lb + m * CV_HS + pos0 * 2;
  const unsigned baddr = lb + CV_U + (r + 32u) * 80u + hi * 16u;
  f32x16 acc0 = {}, acc1 = {};
  for (int D = 0; D < 64; ++D) {
#pragma unroll
    for (int ks = 0; ks < 2; ++ks) {
      const unsigned aa = aaddr + D * 64 - ks * 32;
      const bf16x4 alo = *(const LAS bf16x4*)(size_t)aa, ahi = *(const LAS bf16x4*)(size_t)(aa + 8);
      const bf16x8 a = __builtin_shufflevector(alo, ahi, 0, 1, 2, 3, 4, 5, 6, 7);
      const unsigned bb = baddr - D * 80 + ks * 32;
      const bf16x8 b1 = lds_rd128(bb + 32 * 80);
      acc1 = mfma32(a, b1, acc1);
      if (D < 32) { const bf16x8 b0 = lds_rd128(bb); acc0 = mfma32(a, b0, acc0); }
    }
  }
#pragma unroll
  for (int ct = 0; ct < 2; ++ct)
#pragma unroll
    for (int q4 = 0; q4 < 4; ++q4) {
      const f32x16& a = ct ? acc1 : acc0;
      u32x2 wv = {cvtpk(a[4 * q4], a[4 * q4 + 1]), cvtpk(a[4 * q4 + 2], a[4 * q4 + 3])};
      const int t = 1024 * ct + 32 * (int)r + 8 * q4 + 4 * (int)hi;
      *(u32x2*)(base + CV_Y + t * 2) = wv;
    }
  __syncthreads();
  const float Dc = bf2f(sD[ch]);
#pragma unroll
  for (int it = 0; it < 4; ++it) {
    const int tc = it * 64 + (int)lane, t = tc * 8;
    const u32x4 yv = *(const u32x4*)(base + CV_Y + t * 2);
    const u32x4 uv = *(const u32x4*)(base + CV_U + (32 + (t >> 5)) * 80 + ((t >> 3) & 3) * 16);
    const u32x4 xv = *(const u32x4*)(xT + (size_t)ch * S + t);
    const f32x4 g0 = *(const f32x4*)(gbuf + t), g1 = *(const f32x4*)(gbuf + t + 4);
    float o[8];
#pragma unroll
    for (int e = 0; e < 8; ++e) {
      const float y = (e & 1) ? hi16(yv[e >> 1]) : lo16(yv[e >> 1]);
      const int re = 7 - e;
      const float kv = (re & 1) ? hi16(uv[re >> 1]) : lo16(uv[re >> 1]);
      const float q = (e & 1) ? hi16(xv[e >> 1]) : lo16(xv[e >> 1]);
      const float gg = e < 4 ? g0[e] : g1[e - 4];
      const float y2 = rbf(y + rbf(kv * Dc));
      o[e] = gg * rbf(q * y2);
    }
    u32x4 wv = {cvtpk(o[0], o[1]), cvtpk(o[2], o[3]), cvtpk(o[4], o[5]), cvtpk(o[6], o[7])};
    *(u32x4*)(gaT + (size_t)ch * S + t) = wv;
  }
}

__global__ __launch_bounds__(512, 2) void oproj_kernel(const u16* __restrict__ ctx, const u16* __restrict__ wf, const u16* __restrict__ gaT,
                                                        const float* __restrict__ gbuf, u16* __restrict__ out) {
  extern __shared__ __attribute__((aligned(16))) char lds[];
  const unsigned tid = threadIdx.x, lane = tid & 63u, w = rfl(tid >> 6);
  const unsigned bid = blockIdx.x, xcd = bid & 7u, idx = bid >> 3;
  const unsigned mt = (xcd >> 1) * 8u + (idx & 7u), nt = (xcd & 1u) * 4u + (idx >> 3);
  f32x4 acc[4][5];
#pragma unroll
  for (int i = 0; i < 4; ++i)
#pragma unroll
    for (int j = 0; j < 5; ++j) acc[i][j] = f32x4{};
  const u16* ap = ctx + ((size_t)(mt * 4u) * 128u + w * 16u) * 512u + lane * 8u;
  const u16* bp = wf + ((size_t)(nt * 5u) * 128u + w * 16u) * 512u + lane * 8u;
#pragma unroll 4
  for (int ks = 0; ks < 16; ++ks) {
    bf16x8 a[4], b[5];
#pragma unroll
    for (int i = 0; i < 4; ++i) a[i] = *(const bf16x8*)(ap + (size_t)i * 128 * 512 + ks * 512);
#pragma unroll
    for (int j = 0; j < 5; ++j) b[j] = *(const bf16x8*)(bp + (size_t)j * 128 * 512 + ks * 512);
#pragma unroll
    for (int i = 0; i < 4; ++i)
#pragma unroll
      for (int j = 0; j < 5; ++j) acc[i][j] = mfma16(b[j], a[i], acc[i][j]);
  }
  f32x4* red = (f32x4*)lds;
  if (w >= 4) {
#pragma unroll
    for (int i = 0; i < 4; ++i)
#pragma unroll
      for (int j = 0; j < 5; ++j) red[((w - 4) * 20 + i * 5 + j) * 64 + lane] = acc[i][j];
  }
  __syncthreads();
  if (w < 4) {
#pragma unroll
    for (int i = 0; i < 4; ++i)
#pragma unroll
      for (int j = 0; j < 5; ++j) { acc[i][j] += red[(w * 20 + i * 5 + j) * 64 + lane]; red[(w * 20 + i * 5 + j) * 64 + lane] = acc[i][j]; }
  }
  __syncthreads();
  for (int it = tid; it < 1280; it += 512) {
    const int tile = it >> 6, ln = it & 63, i = tile / 5, j = tile % 5;
    const f32x4 b = red[(0 * 20 + tile) * 64 + ln] + red[(1 * 20 + tile) * 64 + ln] + red[(2 * 20 + tile) * 64 + ln] + red[(3 * 20 + tile) * 64 + ln];
    const int t = mt * 64 + i * 16 + (ln & 15), c = nt * 80 + j * 16 + 4 * (ln >> 4);
    const float omg = gbuf[S + t];
    float o[4];
#pragma unroll
    for (int e = 0; e < 4; ++e) o[e] = bf2f(gaT[(size_t)(c + e) * S + t]) + rbf(omg * rbf(b[e]));
    u32x2 wv = {cvtpk(o[0], o[1]), cvtpk(o[2], o[3])};
    *(u32x2*)(out + (size_t)t * DM + c) = wv;
  }
}

extern "C" void kernel_launch(void* const* d_in, const int* in_sizes, int n_in, void* d_out, int out_size, void* d_ws, size_t ws_size,
                              hipStream_t stream) {
  const u16* A_q = (const u16*)d_in[0];
  const u16* A_k = (const u16*)d_in[1];
  const u16* A_v = (const u16*)d_in[2];
  const u16* A_shk = (const u16*)d_in[3];
  const u16* A_ssmk = (const u16*)d_in[4];
  const u16* A_ssmD = (const u16*)d_in[5];
  const u16* B_ckv = (const u16*)d_in[7];
  const u16* B_wq = (const u16*)d_in[8];
  const u16* B_wuk = (const u16*)d_in[9];
  const u16* B_wuv = (const u16*)d_in[10];
  const u16* B_wo = (const u16*)d_in[11];
  const u16* gate_w = (const u16*)d_in[12];
  char* ws = (char*)d_ws;
  u16* Kg = (u16*)(ws + WS_K); u16* Vg = (u16*)(ws + WS_V); u16* Qg = (u16*)(ws + WS_Q); u16* ctx = (u16*)(ws + WS_CTX);
  u16* wf = (u16*)(ws + WS_WO); u16* uT = (u16*)(ws + WS_UT); u16* xT = (u16*)(ws + WS_XT); u16* gaT = (u16*)(ws + WS_GA);
  float* gbuf = (float*)(ws + WS_G);
  (void)hipFuncSetAttribute((const void*)proj_kernel, hipFuncAttributeMaxDynamicSharedMemorySize, 131072);
  (void)hipFuncSetAttribute((const void*)attn_kernel, hipFuncAttributeMaxDynamicSharedMemorySize, 163840);
  (void)hipFuncSetAttribute((const void*)conv_kernel, hipFuncAttributeMaxDynamicSharedMemorySize, 4 * CV_WAVE);
  (void)hipFuncSetAttribute((const void*)oproj_kernel, hipFuncAttributeMaxDynamicSharedMemorySize, 81920);
  prepass_kernel<<<256, 256, 0, stream>>>(A_q, A_k, A_v, A_shk, gate_w, uT, xT, gbuf);
  wo_frag_kernel<<<1280, 256, 0, stream>>>(B_wo, wf);
  conv_kernel<<<160, 256, 4 * CV_WAVE, stream>>>(A_ssmk, A_ssmD, uT, xT, gbuf, gaT);
  proj_kernel<<<256, 512, 131072, stream>>>(A_q, B_ckv, B_wq, B_wuk, B_wuv, Qg, Kg, Vg);
  attn_kernel<<<256, 512, 163840, stream>>>(Qg, Kg, Vg, ctx);
  oproj_kernel<<<256, 512, 81920, stream>>>(ctx, wf, gaT, gbuf, (u16*)d_out);
}
```

```cpp
#include <hip/hip_runtime.h>
#include <hip/hip_bf16.h>
#include <stdint.h>
#include <type_traits>

#define DI __device__ __forceinline__
#define LAS __attribute__((address_space(3)))
typedef __bf16 bf16x8 __attribute__((ext_vector_type(8)));
typedef __bf16 bf16x4 __attribute__((ext_vector_type(4)));
typedef __bf16 bf16x2 __attribute__((ext_vector_type(2)));
typedef float f32x16 __attribute__((ext_vector_type(16)));
typedef float f32x4 __attribute__((ext_vector_type(4)));
typedef float f32x2 __attribute__((ext_vector_type(2)));
typedef unsigned u32x4 __attribute__((ext_vector_type(4)));
typedef unsigned u32x2 __attribute__((ext_vector_type(2)));
typedef unsigned short u16;

constexpr int S = 2048, DM = 640, NH = 32, HD = 128, DC = 512, NQ = 4096;
constexpr size_t MB = 1u << 20;
constexpr size_t WS_K = 0, WS_V = 16 * MB, WS_Q = 32 * MB, WS_CTX = 48 * MB, WS_WO = 64 * MB, WS_UT = 72 * MB,
                 WS_XT = 76 * MB, WS_GA = 80 * MB, WS_G = 84 * MB, WS_CNT = 85 * MB, WS_PART = 96 * MB, WS_TOTAL = 120 * MB;
#define SRD3 0x00020000
#define ST_AUX 16
#define CTX_AUX 16

DI unsigned rfl(unsigned x) { return __builtin_amdgcn_readfirstlane(x); }
DI unsigned cvtpk(float lo, float hi) { f32x2 v = {lo, hi}; bf16x2 b = __builtin_convertvector(v, bf16x2); return __builtin_bit_cast(unsigned, b); }
DI float rbf(float x) { return (float)(__bf16)x; }
DI float bf2f(u16 v) { return __uint_as_float((unsigned)v << 16); }
DI float lo16(unsigned v) { return __uint_as_float(v << 16); }
DI float hi16(unsigned v) { return __uint_as_float(v & 0xffff0000u); }
DI f32x16 mfma32(bf16x8 a, bf16x8 b, f32x16 c) { return __builtin_amdgcn_mfma_f32_32x32x16_bf16(a, b, c, 0, 0, 0); }
DI f32x4 mfma16(bf16x8 a, bf16x8 b, f32x4 c) { return __builtin_amdgcn_mfma_f32_16x16x32_bf16(a, b, c, 0, 0, 0); }
DI bf16x8 lds_rd128(unsigned addr) { return *(const LAS bf16x8*)(size_t)addr; }
DI bf16x8 lds_rdtr(unsigned addr) {
  bf16x4 lo = __builtin_amdgcn_ds_read_tr16_b64_v4bf16((LAS bf16x4*)(size_t)addr);
  bf16x4 hi = __builtin_amdgcn_ds_read_tr16_b64_v4bf16((LAS bf16x4*)(size_t)(addr + 256));
  return __builtin_shufflevector(lo, hi, 0, 1, 2, 3, 4, 5, 6, 7);
}
#define DMA16(m0v, voff, srd, soff) \
  asm volatile("s_mov_b32 m0, %0\n\ts_nop 0\n\tbuffer_load_dwordx4 %1, %2, %3 offen lds" ::"s"(m0v), "v"(voff), "s"(srd), "s"(soff) : "memory")
#define DMA16_OFF(voff, srd, soff, imm) \
  asm volatile("buffer_load_dwordx4 %0, %1, %2 offen offset:%3 lds" ::"v"(voff), "s"(srd), "s"(soff), "i"(imm) : "memory")
#define SET_M0(m0v) asm volatile("s_mov_b32 m0, %0\n\ts_nop 0" ::"s"(m0v) : "memory")
#define SBAR() __builtin_amdgcn_sched_barrier(0)
#define STAMP(K, idx)
#define STAMPR(K, idx)
#define STEPSTAMP(K, idx)
template <int N, class F> DI void static_for(F&& f) {
  if constexpr (N > 0) { static_for<N - 1>(f); f(std::integral_constant<int, N - 1>{}); }
}
#define PINV(x) asm volatile("" : "+v"(x))
template <int AUX> DI void bst128(u32x4 v, __amdgpu_buffer_rsrc_t r, unsigned voff, unsigned soff) {
  __builtin_amdgcn_raw_buffer_store_b128(v, r, voff, soff, AUX);
  SBAR();
  asm volatile("s_nop 1");
  SBAR();
}
DI u32x4 pack8(const f32x16& a, int s) {
  u32x4 w;
  w[0] = cvtpk(a[8 * s + 0], a[8 * s + 1]); w[1] = cvtpk(a[8 * s + 2], a[8 * s + 3]);
  w[2] = cvtpk(a[8 * s + 4], a[8 * s + 5]); w[3] = cvtpk(a[8 * s + 6], a[8 * s + 7]);
  return w;
}

struct SideArgs { const u16 *x, *k, *v, *shk, *gw, *wo; u16 *uT, *xT, *wf, *gbuf; };
DI u32x4 bld128(__amdgpu_buffer_rsrc_t r, unsigned off) { return __builtin_amdgcn_raw_buffer_load_b128(r, off, 0, 0); }
DI bf16x4 lds_tr4(unsigned addr) { return __builtin_amdgcn_ds_read_tr16_b64_v4bf16((LAS bf16x4*)(size_t)addr); }
constexpr unsigned SD_WO0 = 81920, SD_WO1 = 114688, SD_K = 131072, SD_V = SD_K + 11264, SD_X = SD_V + 10240;
struct SideRegs { u32x4 x0, g0, x1, g1; u32x2 wt[2]; };
DI void side_issue(SideRegs& R, const SideArgs& a, const unsigned bid, const unsigned w, const unsigned lane, const unsigned ldsb) {
  const __amdgpu_buffer_rsrc_t rk = __builtin_amdgcn_make_buffer_rsrc((void*)a.k, (short)0, S * DM * 2, SRD3);
  const __amdgpu_buffer_rsrc_t rv = __builtin_amdgcn_make_buffer_rsrc((void*)a.v, (short)0, S * DM * 2, SRD3);
  const __amdgpu_buffer_rsrc_t rx = __builtin_amdgcn_make_buffer_rsrc((void*)a.x, (short)0, S * DM * 2, SRD3);
  const __amdgpu_buffer_rsrc_t rs = __builtin_amdgcn_make_buffer_rsrc((void*)a.shk, (short)0, DM * 4 * 2, SRD3);
  const __amdgpu_buffer_rsrc_t rg = __builtin_amdgcn_make_buffer_rsrc((void*)a.gw, (short)0, DM * 2, SRD3);
  const __amdgpu_buffer_rsrc_t rwo = __builtin_amdgcn_make_buffer_rsrc((void*)a.wo, (short)0, NQ * DM * 2, SRD3);
  const unsigned t0 = (bid >> 2) * 32u, c0 = (bid & 3u) * 160u;
#pragma unroll
  for (int j = 0; j < 7; ++j) {
    const unsigned i = w + 8u * j;
    if (i < 31u) {
      const unsigned q = i < 11u ? i : i < 21u ? i - 11u : i - 21u;
      const unsigned b = q * 1024u + lane * 16u, row = b / 320u;
      const unsigned vo = row * 1280u + (b - row * 320u);
      if (i < 11u) DMA16(ldsb + SD_K + q * 1024u, vo + (t0 * 1280u + c0 * 2u - 3u * 1280u), rk, 0u);
      else if (i < 21u) DMA16(ldsb + SD_V + q * 1024u, vo, rv, t0 * 1280u + c0 * 2u);
      else DMA16(ldsb + SD_X + q * 1024u, vo, rx, t0 * 1280u + c0 * 2u);
    } else if (i < 51u) {
      const unsigned q = i - 31u, b = q * 1024u + lane * 16u, row = b / 640u;
      DMA16(ldsb + (q < 10u ? SD_WO0 + q * 1024u : SD_WO1 + (q - 10u) * 1024u), row * 1280u + (b - row * 640u), rwo, (bid >> 1) * (32u * 1280u) + (bid & 1u) * 640u);
    }
  }
  R.wt[0] = __builtin_amdgcn_raw_buffer_load_b64(rs, (c0 + w * 16u + (lane & 15u)) * 8u, 0, 0);
  R.wt[1] = __builtin_amdgcn_raw_buffer_load_b64(rs, (c0 + (w + 8u) * 16u + (lane & 15u)) * 8u, 0, 0);
  const unsigned go = (bid * 8u + w) * 1280u;
  R.x0 = bld128(rx, go + lane * 16u); R.g0 = bld128(rg, lane * 16u);
  R.x1 = bld128(rx, lane < 16u ? go + 1024u + lane * 16u : 0x40000000u); R.g1 = bld128(rg, 1024u + lane * 16u);
}
DI void side_gate(const SideRegs& R, const SideArgs& a, const unsigned bid, const unsigned w, const unsigned lane) {
  float s = 0.f;
#pragma unroll
  for (int e = 0; e < 4; ++e)
    s += rbf(lo16(R.x0[e]) * lo16(R.g0[e])) + rbf(hi16(R.x0[e]) * hi16(R.g0[e])) + rbf(lo16(R.x1[e]) * lo16(R.g1[e])) + rbf(hi16(R.x1[e]) * hi16(R.g1[e]));
#pragma unroll
  for (int o = 32; o >= 1; o >>= 1) s += __shfl_xor(s, o);
  if (lane == 0) {
    const unsigned t = bid * 8u + w;
    const float g = rbf(1.f / (1.f + __expf(-rbf(s))));
    a.gbuf[t] = (u16)(__float_as_uint(g) >> 16); a.gbuf[S + t] = (u16)(__float_as_uint(rbf(1.f - g)) >> 16);
  }
}
DI void side_phase2(const SideRegs& R, const SideArgs& a, const unsigned bid, const unsigned w, const unsigned lane, const unsigned ldsb) {
  const unsigned t0 = (bid >> 2) * 32u, c0 = (bid & 3u) * 160u, g = lane >> 4;
  const unsigned la = (8u * g + ((lane >> 2) & 3u)) * 320u + (lane & 3u) * 8u;
#pragma unroll
  for (int rd = 0; rd < 2; ++rd) {
    const unsigned cg = w + 8u * rd;
    if (cg < 10u) {
      const unsigned o = ldsb + la + cg * 32u;
      const u32x2 k0 = __builtin_bit_cast(u32x2, lds_tr4(o + SD_K)), k1 = __builtin_bit_cast(u32x2, lds_tr4(o + SD_K + 4 * 320)), k2 = __builtin_bit_cast(u32x2, lds_tr4(o + SD_K + 8 * 320));
      const u32x2 v0 = __builtin_bit_cast(u32x2, lds_tr4(o + SD_V)), v1 = __builtin_bit_cast(u32x2, lds_tr4(o + SD_V + 4 * 320));
      const u32x2 x0 = __builtin_bit_cast(u32x2, lds_tr4(o + SD_X)), x1 = __builtin_bit_cast(u32x2, lds_tr4(o + SD_X + 4 * 320));
      const float kk[12] = {lo16(k0[0]), hi16(k0[0]), lo16(k0[1]), hi16(k0[1]), lo16(k1[0]), hi16(k1[0]), lo16(k1[1]), hi16(k1[1]), lo16(k2[0]), hi16(k2[0]), lo16(k2[1]), hi16(k2[1])};
      const float vv[8] = {lo16(v0[0]), hi16(v0[0]), lo16(v0[1]), hi16(v0[1]), lo16(v1[0]), hi16(v1[0]), lo16(v1[1]), hi16(v1[1])};
      const float w0 = lo16(R.wt[rd][0]), w1 = hi16(R.wt[rd][0]), w2 = lo16(R.wt[rd][1]), w3 = hi16(R.wt[rd][1]);
      float u[8];
#pragma unroll
      for (int e = 0; e < 8; ++e) u[e] = (kk[e] * w3 + kk[e + 1] * w2 + kk[e + 2] * w1 + kk[e + 3] * w0) * vv[e];
      const size_t go = (size_t)(c0 + cg * 16u + (lane & 15u)) * S + t0 + 8u * g;
      *(u32x4*)(a.uT + go) = u32x4{cvtpk(u[7], u[6]), cvtpk(u[5], u[4]), cvtpk(u[3], u[2]), cvtpk(u[1], u[0])};
      *(u32x4*)(a.xT + go) = u32x4{x0[0], x0[1], x1[0], x1[1]};
    }
  }
  const unsigned ra = ldsb + ((g >> 1) ? SD_WO1 : SD_WO0) + (4u * (g & 1u) + ((lane >> 2) & 3u)) * 640u + (lane & 3u) * 8u;
#pragma unroll
  for (int j = 0; j < 3; ++j) {
    const unsigned f = w + 8u * j;
    if (f < 20u) {
      const bf16x4 lo = lds_tr4(ra + f * 32u), hi = lds_tr4(ra + f * 32u + 8u * 640u);
      *(bf16x8*)(a.wf + ((size_t)(((bid & 1u) * 20u + f) * 128u + (bid >> 1)) * 64u + lane) * 8u) = __builtin_shufflevector(lo, hi, 0, 1, 2, 3, 4, 5, 6, 7);
    }
  }
}

#define PF2MODE 1
struct PCtx {
  unsigned xa[2], xb[2];
  unsigned wa[2], wq[2];
  unsigned vxkv[2], vxq[2], vw;
  unsigned vpf;
  unsigned vk, vv;
  unsigned vpf2;
};
struct PUni {
  unsigned m0x, m0w, m0wq;
  unsigned sxkv, sxq, swo;
  __amdgpu_buffer_rsrc_t rc, rx, rwk, rwv, rwq, rko, rvo;
  __amdgpu_buffer_rsrc_t rpf;
  unsigned pfso, pfstep, pfrole, m0pf;
  __amdgpu_buffer_rsrc_t rpf2;
  unsigned pf2so;
};
DI void p_pf(const PCtx& c, const PUni& u, unsigned so) {
  asm volatile("s_mov_b32 m0, %0\n\ts_nop 0\n\tbuffer_load_dword %1, %2, %3 offen lds" ::"s"(u.m0pf), "v"(c.vpf), "s"(u.rpf), "s"(so) : "memory");
}
DI void p_pf2(const PCtx& c, const PUni& u, unsigned so) {
  asm volatile("s_mov_b32 m0, %0\n\ts_nop 0\n\tbuffer_load_dword %1, %2, %3 offen lds" ::"s"(PF2MODE == 1 ? u.m0pf + 2048u : u.m0pf - 4096u), "v"(c.vpf2), "s"(u.rpf2), "s"(so) : "memory");
}
struct PRegs { bf16x8 w[4]; bf16x8 x[5]; u32x4 pk[8]; };
constexpr int p_xslot(int U) { return U < 8 ? (U & 1) * 32768 : ((U - 8) % 3 == 0 ? 0 : (U - 8) % 3 == 1 ? 131072 : 32768); }
constexpr int p_wslot(int U) { return U < 8 ? (U & 1) * 32768 : ((U - 8) % 3) * 16384; }
template <int U, int J> DI void p_dma_x(const PCtx& c, const PUni& u) {
  constexpr int K = U < 8 ? U : U - 8;
  if constexpr (U < 8) DMA16(u.m0x + p_xslot(U) + J * 1024, c.vxkv[J >> 1], u.rc, u.sxkv + J * 8192 + K * 128);
  else if constexpr (U < 18) DMA16(u.m0x + p_xslot(U) + J * 1024, c.vxq[J >> 1], u.rx, u.sxq + J * (8 * 1280) + K * 128);
}
template <int U, int J> DI void p_dma_w(const PCtx& c, const PUni& u) {
  constexpr int K = U < 8 ? U : U - 8;
  if constexpr (U < 8) {
    if constexpr (J < 2) DMA16(u.m0w + p_wslot(U) + J * 1024, c.vw, u.rwk, u.swo + (J & 1) * 128 + K * (64 * 8192));
    else                 DMA16(u.m0w + p_wslot(U) + J * 1024, c.vw, u.rwv, u.swo + (J & 1) * 128 + K * (64 * 8192));
  } else if constexpr (U < 18) DMA16(u.m0wq + p_wslot(U) + J * 1024, c.vw, u.rwq, u.swo + J * 128 + K * (64 * 8192));
}
template <int U, int T> DI bf16x8 p_rdx(const PCtx& c) {
  constexpr int xs = p_xslot(U);
  if constexpr (xs < 65536) return lds_rd128(c.xa[T >> 3] + xs + ((T & 7) >> 1) * 4096 + (T & 1) * 1024);
  else                      return lds_rd128(c.xb[T >> 3] + ((T & 7) >> 1) * 4096 + (T & 1) * 1024);
}
template <int U, int KS, int N> DI bf16x8 p_rdw(const PCtx& c) {
  if constexpr (U < 8) return lds_rdtr(c.wa[N & 1] + p_wslot(U) + KS * 16384 + (N >> 1) * 2048);
  else                 return lds_rdtr(c.wq[N & 1] + p_wslot(U) + KS * 8192);
}
constexpr int p_vm(int U) {
  return PF2MODE == 2 ? (U < 7 ? 1 : U == 7 ? 2 : U == 8 ? 9 : U < 16 ? 12 : 4) : (U < 7 ? (PF2MODE == 1 ? 2 : 1) : U == 7 ? 2 : U == 8 ? 8 : U < 16 ? 10 : 2);
}
template <int U> DI void p_step(PRegs& R, f32x4 (&acc)[8][4], const PCtx& c, const PUni& u) {
  constexpr bool KV = U < 8;
  constexpr int NW = KV ? 4 : 2, NWN = U + 1 < 8 ? 4 : 2;
  bf16x8 x[16], w0[4], w1[4];
#pragma unroll
  for (int i = 0; i < 5; ++i) x[i] = R.x[i];
#pragma unroll
  for (int n = 0; n < NW; ++n) w0[n] = R.w[n];
  static_for<16>([&](auto gc) {
    constexpr int G = decltype(gc)::value;
    constexpr int I = G & 7;
    if constexpr (G == 12 && U < 17) {
      asm volatile("s_waitcnt vmcnt(%0) lgkmcnt(0)\n\ts_barrier" ::"i"(p_vm(U)) : "memory");
      SBAR();
    }
    static_for<NW>([&](auto nc) {
      constexpr int N = decltype(nc)::value;
      const bf16x8 wf = G < 8 ? w0[N] : w1[N];
      if constexpr (N < 2) acc[I][N] = mfma16(wf, x[G], (U == 8 && G < 8) ? f32x4{} : acc[I][N]);
      else                 acc[I][N] = mfma16(x[G], wf, acc[I][N]);
    });
    if constexpr (G + 5 < 16) x[G + 5] = p_rdx<U, G + 5>(c);
    if constexpr (NW == 4) { if constexpr (G < 8 && (G & 1) == 0) w1[G >> 1] = p_rdw<U, 1, (G >> 1)>(c); }
    else { if constexpr (G == 0 || G == 2) w1[G >> 1] = p_rdw<U, 1, (G >> 1)>(c); }
    if constexpr (U < 17) {
      if constexpr (G == 12) { R.w[0] = p_rdw<U + 1, 0, 0>(c); R.w[1] = p_rdw<U + 1, 0, 1>(c); R.x[0] = p_rdx<U + 1, 0>(c); }
      if constexpr (G == 13) { if constexpr (NWN == 4) { R.w[2] = p_rdw<U + 1, 0, 2>(c); R.w[3] = p_rdw<U + 1, 0, 3>(c); } R.x[1] = p_rdx<U + 1, 1>(c); }
      if constexpr (G == 14) { R.x[2] = p_rdx<U + 1, 2>(c); R.x[3] = p_rdx<U + 1, 3>(c); }
      if constexpr (G == 15) { R.x[4] = p_rdx<U + 1, 4>(c); }
    }
    if constexpr (U < 7) { if constexpr (G < 4) p_dma_w<U + 1, G>(c, u); }
    else if constexpr (U == 7) { if constexpr (G < 4) p_dma_w<8 + (G >> 1), G & 1>(c, u); }
    else { if constexpr (G < 2) p_dma_w<U + 2, G>(c, u); }
    if constexpr (U < 7) { if constexpr (G >= 12) p_dma_x<U + 2, G - 12>(c, u); }
    else if constexpr (U == 7) { if constexpr (G >= 12) { p_dma_x<9 + ((G - 12) >> 1), 2 * (G & 1)>(c, u); p_dma_x<9 + ((G - 12) >> 1), 2 * (G & 1) + 1>(c, u); } }
    else { if constexpr (G >= 12) p_dma_x<U + 3, G - 12>(c, u); }
    if constexpr (G == 4 && U < 7) {
      const unsigned t = u.pfrole < 2u ? (unsigned)U + 3u : (unsigned)U;
      p_pf(c, u, t <= 7u ? u.pfso + t * u.pfstep : 0x40000000u);
    }
    if constexpr (PF2MODE == 2 && G == 2 && U >= 8 && U < 17) p_pf2(c, u, u.pf2so + (U - 8) * 896u);
    if constexpr (PF2MODE == 1 && G == 6 && U < 7) p_pf2(c, u, u.pf2so + U * 1152u);
    if constexpr (U >= 8 && U < 16 && (G == 6 || G == 7)) {
      constexpr int q = 2 * (U - 8) + (G - 6);
      if constexpr (q < 8) bst128<ST_AUX>(R.pk[q], u.rko, c.vk, (q >> 1) * 8192 + (q & 1) * 128);
      else {
        constexpr int a = (q - 8) >> 1, n = 2 + (q & 1);
        const u32x4 vv = {cvtpk(acc[2 * a][n][0], acc[2 * a][n][1]), cvtpk(acc[2 * a][n][2], acc[2 * a][n][3]),
                          cvtpk(acc[2 * a + 1][n][0], acc[2 * a + 1][n][1]), cvtpk(acc[2 * a + 1][n][2], acc[2 * a + 1][n][3])};
        bst128<ST_AUX>(vv, u.rvo, c.vv, a * 8192 + (n - 2) * 256);
      }
    }
    SBAR();
  });
}

__global__ __launch_bounds__(512, 2) void proj_kernel(const u16* __restrict__ x, const u16* __restrict__ ckv, const u16* __restrict__ wq,
                                                       const u16* __restrict__ wuk, const u16* __restrict__ wuv, u16* __restrict__ Qg,
                                                       u16* __restrict__ Kg, u16* __restrict__ Vg, unsigned* __restrict__ cnt, const SideArgs sa, const u16* __restrict__ hk) {
  extern __shared__ __attribute__((aligned(16))) char lds[];
  const unsigned tid = threadIdx.x, lane = tid & 63u, w = rfl(tid >> 6);
  const unsigned bid = blockIdx.x, idx = bid >> 3, h = (bid & 7u) * 4u + (idx >> 3), p = idx & 7u;
  const unsigned wm = w >> 2, wn = w & 3u;
  const unsigned ldsb = (unsigned)(size_t)(LAS char*)lds;
  PCtx c; PUni u;
  {
    const unsigned rho = lane & 15u, kg = lane >> 4;
    const unsigned kap = 16u * (rho >> 3) + 4u * ((rho >> 2) & 1u) + (rho & 3u);
    const unsigned bx = ldsb + (128u * wm + kap) * 128u + ((kg ^ (rho >> 1)) << 4);
    c.xa[0] = bx; c.xa[1] = bx ^ 64u; c.xb[0] = c.xa[0] + 131072u; c.xb[1] = c.xa[1] + 131072u;
    const unsigned bl = ((lane >> 2) & 3u) * 64u + (lane & 3u) * 8u + wn * 512u + ldsb + 65536u;
    c.wa[0] = bl + kg * 4096u + (kg & 1u) * 32u; c.wa[1] = bl + kg * 4096u + ((kg & 1u) ^ 1u) * 32u;
    c.wq[0] = bl + kg * 2048u + (kg & 1u) * 32u; c.wq[1] = bl + kg * 2048u + ((kg & 1u) ^ 1u) * 32u;
#pragma unroll
    for (int par = 0; par < 2; ++par) {
      const unsigned ch = ((lane & 7u) ^ ((unsigned)(par << 2) | (((lane >> 5) & 1u) << 1) | ((lane >> 4) & 1u))) * 16u;
      c.vxkv[par] = (lane >> 3) * 1024u + ch;
      c.vxq[par] = (lane >> 3) * 1280u + ch;
    }
    c.vw = ((lane >> 2) & 7u) * 8192u + (lane >> 5) * 64u + ((lane & 3u) ^ (2u * (w & 1u))) * 16u;
    c.vk = ((2u * wn + (kg >> 1)) * 64u + (kg & 1u) * 32u + kap) * 16u;
    c.vv = (((kg >> 1) * 4u + wn) * 64u + (kg & 1u) * 32u + rho) * 16u;
    PINV(c.xa[0]); PINV(c.xa[1]); PINV(c.xb[0]); PINV(c.xb[1]); PINV(c.wa[0]); PINV(c.wa[1]); PINV(c.wq[0]); PINV(c.wq[1]);
    u.m0x = ldsb + w * 4096u; u.m0w = ldsb + 65536u + w * 4096u; u.m0wq = ldsb + 65536u + w * 2048u;
    u.sxkv = (256u * p + 32u * w) * 1024u;
    u.sxq = ((w < 4 ? 128u * p + 32u * w : 128u * (15u - p) + 32u * (w - 4u))) * 1280u;
    u.swo = (8u * w) * 8192u + h * 256u;
    u.rc = __builtin_amdgcn_make_buffer_rsrc((void*)ckv, (short)0, S * DC * 2, SRD3);
    u.rx = __builtin_amdgcn_make_buffer_rsrc((void*)x, (short)0, S * DM * 2, SRD3);
    u.rwk = __builtin_amdgcn_make_buffer_rsrc((void*)wuk, (short)0, DC * NQ * 2, SRD3);
    u.rwv = __builtin_amdgcn_make_buffer_rsrc((void*)wuv, (short)0, DC * NQ * 2, SRD3);
    u.rwq = __builtin_amdgcn_make_buffer_rsrc((void*)wq, (short)0, DM * NQ * 2, SRD3);
    const unsigned ktb = (h * 64u + 8u * p + 4u * wm) * 8192u;
    u.rko = __builtin_amdgcn_make_buffer_rsrc((char*)Kg + ktb, (short)0, 4 * 8192, SRD3);
    u.rvo = __builtin_amdgcn_make_buffer_rsrc((char*)Vg + ktb, (short)0, 4 * 8192, SRD3);
    const unsigned role = w & 3u, half = w >> 2;
    u.pfrole = role; u.m0pf = ldsb + 131072u + w * 256u;
    const void* pfp = role == 0 ? (const void*)wuk : role == 1 ? (const void*)wuv : role == 2 ? (const void*)wq : (const void*)x;
    const int pfn = role < 2 ? DC * NQ * 2 : role == 2 ? DM * NQ * 2 : S * DM * 2;
    u.rpf = __builtin_amdgcn_make_buffer_rsrc((void*)pfp, (short)0, pfn, SRD3);
    if (role < 2)       { c.vpf = lane < 8u ? (lane >> 1) * 8192u + (lane & 1u) * 128u : 0x40000000u; u.pfso = (8u * p + 4u * half) * 8192u + h * 256u; u.pfstep = 64u * 8192u; }
    else if (role == 2) { c.vpf = lane < 12u ? (lane >> 1) * 8192u + (lane & 1u) * 128u : 0x40000000u; u.pfso = (80u * p + 6u * half) * 8192u + h * 256u; u.pfstep = 12u * 8192u; }
    else                { c.vpf = lane < 6u ? lane * 128u : 0x40000000u; u.pfso = (bid * 80u + 6u * half) * 128u; u.pfstep = 12u * 128u; }
    const unsigned gw = bid * 8u + w;
    const void* p2 = gw < 326u ? (const void*)sa.k : gw < 652u ? (const void*)sa.v : gw < 1303u ? (const void*)sa.wo : (const void*)hk;
    const unsigned g2 = gw < 326u ? gw : gw < 652u ? gw - 326u : gw < 1303u ? gw - 652u : gw - 1303u;
    u.rpf2 = __builtin_amdgcn_make_buffer_rsrc((void*)p2, (short)0, (gw >= 652u && gw < 1303u) ? NQ * DM * 2 : S * DM * 2, SRD3);
    u.pf2so = g2 * (63u * 128u);
    c.vpf2 = lane < (PF2MODE == 1 ? 9u : 7u) ? lane * 128u : 0x40000000u;
  }
  STAMPR(0, 38); STAMP(0, 0);
  f32x4 acc[8][4];
#pragma unroll
  for (int i = 0; i < 8; ++i)
#pragma unroll
    for (int n = 0; n < 4; ++n) acc[i][n] = f32x4{};
  PRegs R;
  static_for<4>([&](auto jc) { p_dma_x<0, decltype(jc)::value>(c, u); });
  static_for<4>([&](auto jc) { p_dma_w<0, decltype(jc)::value>(c, u); });
  static_for<4>([&](auto jc) { p_dma_x<1, decltype(jc)::value>(c, u); });
  p_pf(c, u, u.pfrole < 2u ? u.pfso + u.pfstep : 0x40000000u);
  p_pf(c, u, u.pfrole < 2u ? u.pfso + 2u * u.pfstep : 0x40000000u);
  asm volatile("s_waitcnt vmcnt(6)\n\ts_barrier" ::: "memory");
  SBAR();
  static_for<4>([&](auto nc) { R.w[decltype(nc)::value] = p_rdw<0, 0, decltype(nc)::value>(c); });
  static_for<5>([&](auto ic) { R.x[decltype(ic)::value] = p_rdx<0, decltype(ic)::value>(c); });
  STAMP(0, 1);
  static_for<8>([&](auto uc) { p_step<decltype(uc)::value>(R, acc, c, u); });
  STAMP(0, 2);
#pragma unroll
  for (int i = 0; i < 8; ++i)
    R.pk[i] = u32x4{cvtpk(acc[i][0][0], acc[i][0][1]), cvtpk(acc[i][0][2], acc[i][0][3]), cvtpk(acc[i][1][0], acc[i][1][1]), cvtpk(acc[i][1][2], acc[i][1][3])};
  SBAR();
  STAMP(0, 3);
  static_for<9>([&](auto uc) { p_step<8 + decltype(uc)::value>(R, acc, c, u); });
  SideRegs sr;
  side_issue(sr, sa, bid, w, lane, ldsb);
  SBAR();
  p_step<17>(R, acc, c, u);
  STAMP(0, 4);
  {
    unsigned ln = tid & 63u; PINV(ln);
    const unsigned cc = ln & 15u, g = ln >> 4;
    const unsigned kap = 16u * (cc >> 3) + 4u * ((cc >> 2) & 1u) + (cc & 3u);
    const float sc = 0.08838834764831845f * 1.4426950408889634f;
    const __amdgpu_buffer_rsrc_t rqo = __builtin_amdgcn_make_buffer_rsrc((char*)Qg + (h * 64u + (wm == 0 ? 4u * p : 4u * (15u - p))) * 8192u, (short)0, 4 * 8192, SRD3);
    const unsigned vq = ((2u * wn + (g >> 1)) * 64u + (g & 1u) * 32u + kap) * 16u;
#pragma unroll
    for (int i = 0; i < 8; ++i) {
      const f32x4 a = acc[i][0] * sc, b = acc[i][1] * sc;
      u32x4 qv = {cvtpk(a[0], a[1]), cvtpk(a[2], a[3]), cvtpk(b[0], b[1]), cvtpk(b[2], b[3])};
      bst128<ST_AUX>(qv, rqo, vq, (i >> 1) * 8192 + (i & 1) * 128);
    }
  }
  if (tid == 0) cnt[bid * 64u] = 0u;
  STAMP(0, 5);
  side_gate(sr, sa, bid, w, lane);
  asm volatile("s_waitcnt vmcnt(0) lgkmcnt(0)\n\ts_barrier" ::: "memory");
  SBAR();
  STAMP(0, 6);
  side_phase2(sr, sa, bid, w, lane, ldsb);
  STAMP(0, 7); STAMPR(0, 39);
}

struct ConvArgs { const u16 *hk, *sD, *uT, *xT, *gbuf; u16* gaT; };
constexpr unsigned CV_HS = 4416;
constexpr unsigned CV_U = 4 * CV_HS;
constexpr unsigned CV_SLOT = 25600;
constexpr unsigned CV_EX = 3 * CV_SLOT;
struct ConvRegs { u32x4 hd[2], uv0[3], exv[2], eg[2]; u32x2 hn[2]; float eD[2]; };
DI unsigned conv_chan(unsigned bid, unsigned j) { return (bid >> 1) * 5u + (j < 2u ? 2u * (bid & 1u) + j : 4u); }
DI void conv_issue(ConvRegs& R, const ConvArgs& a, const unsigned bid, const unsigned tid) {
  const unsigned odd = bid & 1u;
  const __amdgpu_buffer_rsrc_t rh = __builtin_amdgcn_make_buffer_rsrc((void*)a.hk, (short)0, DM * S * 2, SRD3);
  const __amdgpu_buffer_rsrc_t ru = __builtin_amdgcn_make_buffer_rsrc((void*)a.uT, (short)0, DM * S * 2, SRD3);
  const __amdgpu_buffer_rsrc_t rxt = __builtin_amdgcn_make_buffer_rsrc((void*)a.xT, (short)0, DM * S * 2, SRD3);
  const __amdgpu_buffer_rsrc_t rgb = __builtin_amdgcn_make_buffer_rsrc((void*)a.gbuf, (short)0, S * 2, SRD3);
  const __amdgpu_buffer_rsrc_t rsd = __builtin_amdgcn_make_buffer_rsrc((void*)a.sD, (short)0, DM * 2, SRD3);
  constexpr unsigned OOB = 0x40000000u;
#pragma unroll
  for (int rd = 0; rd < 2; ++rd) {
    const unsigned it = tid + 512u * rd, j = it / 261u;
    const int ip = (int)(it - j * 261u) - 4;
    const unsigned base = conv_chan(bid, j) * 4096u + (unsigned)(ip * 16);
    R.hd[rd] = bld128(rh, (it < 783u && ip >= 0 && ip < 256) ? base : OOB);
    R.hn[rd] = __builtin_amdgcn_raw_buffer_load_b64(rh, (it < 783u && ip >= -1 && ip < 255) ? base + 16u : OOB, 0, 0);
  }
#pragma unroll
  for (int rd = 0; rd < 3; ++rd) {
    const unsigned it = tid + 512u * rd, j = it / 384u, q = it - j * 384u;
    R.uv0[rd] = bld128(ru, (it < 1152u && q >= 128u) ? conv_chan(bid, j) * 4096u + (q - 128u) * 16u : OOB);
  }
#pragma unroll
  for (int rd = 0; rd < 2; ++rd) {
    const unsigned it = tid + 512u * rd, j = it >> 8, t = (j < 2u ? (it & 255u) : odd * 128u + (it & 127u)) * 8u;
    const unsigned c = conv_chan(bid, j < 3u ? j : 2u);
    R.exv[rd] = bld128(rxt, c * 4096u + t * 2u);
    R.eg[rd] = bld128(rgb, t * 2u);
    R.eD[rd] = bf2f(__builtin_amdgcn_raw_buffer_load_b16(rsd, c * 2u, 0, 0));
  }
}
template <int SL> DI void conv_rd(u32x2& al, u32x2& ah, u32x4& bf, unsigned aa, unsigned bb) {
  constexpr int d = SL >> 1, ks = SL & 1;
  asm volatile("ds_read_b64 %0, %3 offset:%5\n\tds_read_b64 %1, %3 offset:%6\n\tds_read_b128 %2, %4 offset:%7"
               : "=&v"(al), "=&v"(ah), "=&v"(bf) : "v"(aa), "v"(bb), "i"(d * 64 + (1 - ks) * 32), "i"(d * 64 + (1 - ks) * 32 + 8), "i"((31 - d) * 80 + ks * 32) : "memory");
}
template <int N> DI void conv_wait(u32x2& al, u32x2& ah, u32x4& bf) { asm volatile("s_waitcnt lgkmcnt(%3)" : "+v"(al), "+v"(ah), "+v"(bf) : "i"(N) : "memory"); }
DI void conv_run(const ConvRegs& R, const ConvArgs& a, const unsigned bid, const unsigned tid, const unsigned cvb) {
  const unsigned lane = tid & 63u, w = rfl(tid >> 6), odd = bid & 1u;
#pragma unroll
  for (int rd = 0; rd < 2; ++rd) {
    const unsigned it = tid + 512u * rd;
    if (it < 783u) {
      const unsigned j = it / 261u, i = it - j * 261u;
      const u32x4 d = R.hd[rd]; const u32x2 n = R.hn[rd];
      const unsigned a10 = __builtin_amdgcn_alignbit(d[1], d[0], 16), a21 = __builtin_amdgcn_alignbit(d[2], d[1], 16), a32 = __builtin_amdgcn_alignbit(d[3], d[2], 16),
                     an3 = __builtin_amdgcn_alignbit(n[0], d[3], 16), ann = __builtin_amdgcn_alignbit(n[1], n[0], 16);
      const unsigned bp = cvb + j * CV_SLOT + i * 16u;
      *(LAS u32x4*)(size_t)(bp) = u32x4{d[2], d[3], n[0], n[1]};
      *(LAS u32x4*)(size_t)(bp + CV_HS) = u32x4{a10, a21, a32, an3};
      *(LAS u32x4*)(size_t)(bp + 2 * CV_HS) = u32x4{d[1], d[2], d[3], n[0]};
      *(LAS u32x4*)(size_t)(bp + 3 * CV_HS) = u32x4{a21, a32, an3, ann};
    }
  }
#pragma unroll
  for (int rd = 0; rd < 3; ++rd) {
    const unsigned it = tid + 512u * rd;
    if (it < 1152u) {
      const unsigned j = it / 384u, q = it - j * 384u;
      *(LAS u32x4*)(size_t)(cvb + j * CV_SLOT + CV_U + (q >> 2) * 80u + (q & 3u) * 16u) = R.uv0[rd];
    }
  }
  asm volatile("s_waitcnt lgkmcnt(0)\n\ts_barrier" ::: "memory");
  STAMP(1, 25);
  const unsigned j = w < 3u ? 0u : w < 6u ? 1u : 2u;
  const unsigned unit = w < 6u ? w - 3u * j : (w - 6u) + odd + 3u * ((w - 6u) & (odd ^ 1u));
  const unsigned r = lane & 31u, hi = lane >> 5, m = (r + 1u) & 3u;
  const unsigned sb = cvb + j * CV_SLOT;
  f32x16 acc = {};
  if (unit < 3u) {
    const unsigned D0 = unit == 2u ? 32u : 0u, ct = unit == 0u ? 0u : 1u;
    const unsigned aa = sb + m * CV_HS + (unsigned)((int)r - 8 * (int)hi - 7 + (m == 0u ? 28 : 32 - (int)m) - 16) * 2u + D0 * 64u;
    const unsigned bb = sb + CV_U + (32u * ct + r + 32u - D0 - 31u) * 80u + hi * 16u;
    constexpr int PD = 3;
    u32x2 al[64], ah[64]; u32x4 bf[64];
    auto rd = [&](auto sc) { constexpr int sl = decltype(sc)::value; conv_rd<sl>(al[sl], ah[sl], bf[sl], aa, bb); };
    static_for<PD>([&](auto sc) { rd(sc); });
    static_for<64>([&](auto sc) {
      constexpr int sl = decltype(sc)::value, left = 63 - sl < PD - 1 ? 63 - sl : PD - 1;
      conv_wait<3 * left>(al[sl], ah[sl], bf[sl]);
      SBAR();
      const u32x4 av = {al[sl][0], al[sl][1], ah[sl][0], ah[sl][1]};
      acc = mfma32(__builtin_bit_cast(bf16x8, av), __builtin_bit_cast(bf16x8, bf[sl]), acc);
      if constexpr (sl + PD < 64) rd(std::integral_constant<int, sl + PD>{});
      SBAR();
    });
  }
  STAMP(1, 26);
  LAS f32x4* ex = (LAS f32x4*)(size_t)(cvb + CV_EX + j * 4096u + lane * 16u);
  if (unit == 2u) {
#pragma unroll
    for (int q4 = 0; q4 < 4; ++q4) ex[q4 * 64] = f32x4{acc[4 * q4], acc[4 * q4 + 1], acc[4 * q4 + 2], acc[4 * q4 + 3]};
  }
  asm volatile("s_waitcnt lgkmcnt(0)\n\ts_barrier" ::: "memory");
  if (unit < 2u) {
#pragma unroll
    for (int q4 = 0; q4 < 4; ++q4) {
      f32x4 v = {acc[4 * q4], acc[4 * q4 + 1], acc[4 * q4 + 2], acc[4 * q4 + 3]};
      if (unit == 1u) v += ex[q4 * 64];
      *(LAS u32x2*)(size_t)(sb + (1024u * unit + 32u * r + 8u * q4 + 4u * hi) * 2u) = u32x2{cvtpk(v[0], v[1]), cvtpk(v[2], v[3])};
    }
  }
  asm volatile("s_waitcnt lgkmcnt(0)\n\ts_barrier" ::: "memory");
  STAMP(1, 27);
#pragma unroll
  for (int rd = 0; rd < 2; ++rd) {
    const unsigned it = tid + 512u * rd, jj = it >> 8, t = (jj < 2u ? (it & 255u) : odd * 128u + (it & 127u)) * 8u;
    if (it < 640u) {
      const unsigned s2 = cvb + jj * CV_SLOT;
      const u32x4 yv = *(const LAS u32x4*)(size_t)(s2 + t * 2u);
      const u32x4 uv = *(const LAS u32x4*)(size_t)(s2 + CV_U + (32u + (t >> 5)) * 80u + ((t >> 3) & 3u) * 16u);
      float o[8];
#pragma unroll
      for (int e = 0; e < 8; ++e) {
        const float y = (e & 1) ? hi16(yv[e >> 1]) : lo16(yv[e >> 1]);
        const int re = 7 - e;
        const float kv = (re & 1) ? hi16(uv[re >> 1]) : lo16(uv[re >> 1]);
        const float q = (e & 1) ? hi16(R.exv[rd][e >> 1]) : lo16(R.exv[rd][e >> 1]);
        const float gg = (e & 1) ? hi16(R.eg[rd][e >> 1]) : lo16(R.eg[rd][e >> 1]);
        o[e] = gg * (q * (y + kv * R.eD[rd]));
      }
      *(u32x4*)(a.gaT + (size_t)conv_chan(bid, jj) * S + t) = u32x4{cvtpk(o[0], o[1]), cvtpk(o[2], o[3]), cvtpk(o[4], o[5]), cvtpk(o[6], o[7])};
    }
  }
}

constexpr float THR = 8.f;
struct AState {
  f32x16 O[4];
  bf16x8 Q[8];
  f32x16 S[2][2];
  f32x16 negm;
  unsigned P[4][4];
  bf16x8 kf[4];
  float mhat, l0, l1, mA;
  unsigned kaddr, vaddr;
};
struct AUni {
  unsigned vlin;
  unsigned m0k, m0v;
  unsigned sk;
  unsigned ldsb, w, h, g;
  int n0;
  __amdgpu_buffer_rsrc_t rk, rv, rq;
  u16* ctx;
};
DI float max3f(float a, float b, float c) { return __builtin_fmaxf(__builtin_fmaxf(a, b), c); }
DI int a_pair(const AUni& un, int u) { u = u > 16 ? 16 : u; return u < un.n0 ? u : u - un.n0; }
DI void a_dma_k(const AUni& st, int s, int slot) {
  SET_M0(st.m0k + (unsigned)slot * 32768u);
  const unsigned so = st.sk + (unsigned)s * 32768u;
  DMA16_OFF(st.vlin, st.rk, so, 0); DMA16_OFF(st.vlin, st.rk, so, 1024); DMA16_OFF(st.vlin, st.rk, so, 2048); DMA16_OFF(st.vlin, st.rk, so, 3072);
}
DI bf16x8 pfrag(const unsigned (&p)[4]) { u32x4 w = {p[0], p[1], p[2], p[3]}; return __builtin_bit_cast(bf16x8, w); }
DI void mask16(f32x16& s, int dq, int add) {
  const float ninf = -__builtin_inff();
#pragma unroll
  for (int e = 0; e < 16; e += 4) {
    const int cr = 2 * e + add;
    float x0 = s[e], x1 = s[e + 1], x2 = s[e + 2], x3 = s[e + 3];
    unsigned long long m0, m1, m2, m3;
    asm volatile("v_cmp_gt_i32 %4, %9, %8\n\tv_cmp_gt_i32 %5, %10, %8\n\tv_cmp_gt_i32 %6, %11, %8\n\tv_cmp_gt_i32 %7, %12, %8\n\t"
                 "v_cndmask_b32 %0, %0, %13, %4\n\tv_cndmask_b32 %1, %1, %13, %5\n\tv_cndmask_b32 %2, %2, %13, %6\n\ts_nop 0\n\tv_cndmask_b32 %3, %3, %13, %7"
                 : "+v"(x0), "+v"(x1), "+v"(x2), "+v"(x3), "=&s"(m0), "=&s"(m1), "=&s"(m2), "=&s"(m3)
                 : "v"(dq), "i"(cr), "i"(cr + 1), "i"(cr + 2), "i"(cr + 3), "v"(ninf));
    s[e] = x0; s[e + 1] = x1; s[e + 2] = x2; s[e + 3] = x3;
  }
}
DI void sub16(f32x16& s, float d) {
#pragma unroll
  for (int e = 0; e < 16; ++e) { float x = s[e]; asm volatile("v_sub_f32 %0, %0, %1" : "+v"(x) : "v"(d)); s[e] = x; }
}
DI void bcast16(f32x16& s, float d) {
#pragma unroll
  for (int e = 0; e < 16; ++e) { float x = s[e]; asm volatile("v_mov_b32 %0, %1" : "+v"(x) : "v"(d)); s[e] = x; }
}
DI void mul16(f32x16& s, float d) {
#pragma unroll
  for (int e = 0; e < 16; ++e) { float x = s[e]; asm volatile("v_mul_legacy_f32 %0, %0, %1" : "+v"(x) : "v"(d)); s[e] = x; }
}
#define AEXP(x) __builtin_amdgcn_exp2f(x)
DI bf16x8 a_rd(unsigned addr) { return lds_rd128(addr); }
#define WG_BAR() do { asm volatile("s_waitcnt lgkmcnt(0)\n\ts_barrier" ::: "memory"); SBAR(); } while (0)

DI void a_epilogue(AState& st, const AUni& un, const float m, const float lsum, const int blk, const unsigned xa, const unsigned xb) {
  unsigned lane = threadIdx.x & 63u; PINV(lane);
  const unsigned r = lane & 31u, hi = lane >> 5, w = un.w, g = w >> 2, rw = w & 3u;
  auto pr = __builtin_amdgcn_permlane32_swap(__float_as_uint(lsum), __float_as_uint(lsum), false, false);
  const float lt = __uint_as_float(pr[0]) + __uint_as_float(pr[1]);
  LAS float* ml = (LAS float*)(size_t)xb;
  ml[w * 64 + lane] = hi ? lt : m;
  WG_BAR();
  const unsigned wo = w ^ 4u;
  const float mo = ml[wo * 64 + r], lo = ml[wo * 64 + 32 + r];
  const float ms = __builtin_fmaxf(m, mo);
  const float ws_ = __builtin_amdgcn_exp2f(m - ms), wo_ = __builtin_amdgcn_exp2f(mo - ms);
  const float sc = ws_ * __builtin_amdgcn_rcpf(lt * ws_ + lo * wo_);
  WG_BAR();
  LAS f32x4* xs = (LAS f32x4*)(size_t)((g ? xb : xa) + rw * 8192u + lane * 16u);
  LAS f32x4* xr = (LAS f32x4*)(size_t)((g ? xa : xb) + rw * 8192u + lane * 16u);
  if (g == 0) {
#pragma unroll
    for (int q4 = 0; q4 < 8; ++q4) { f32x4 t;
#pragma unroll
      for (int e = 0; e < 4; ++e) t[e] = st.O[2 + (q4 >> 2)][4 * (q4 & 3) + e] * sc;
      xs[q4 * 64] = t; }
  } else {
#pragma unroll
    for (int q4 = 0; q4 < 8; ++q4) { f32x4 t;
#pragma unroll
      for (int e = 0; e < 4; ++e) t[e] = st.O[q4 >> 2][4 * (q4 & 3) + e] * sc;
      xs[q4 * 64] = t; }
  }
  WG_BAR();
  const __amdgpu_buffer_rsrc_t rco = __builtin_amdgcn_make_buffer_rsrc((char*)un.ctx + (size_t)((unsigned)blk * 8u + rw * 2u) * 131072u, (short)0, 2 * 131072, SRD3);
  const unsigned vc = (((r >> 4) * 128u + un.h * 4u + 2u * g) * 4u + hi) * 256u + (r & 15u) * 16u;
#pragma unroll
  for (int dd = 0; dd < 2; ++dd) {
    f32x16 o;
#pragma unroll
    for (int q4 = 0; q4 < 4; ++q4) {
      const f32x4 t = xr[(dd * 4 + q4) * 64];
#pragma unroll
      for (int e = 0; e < 4; ++e) o[4 * q4 + e] = (g == 0 ? st.O[dd][4 * q4 + e] : st.O[2 + dd][4 * q4 + e]) * sc + t[e];
    }
#pragma unroll
    for (int s2 = 0; s2 < 2; ++s2) bst128<CTX_AUX>(pack8(o, s2), rco, vc, (dd * 4 + 2 * s2) * 256);
  }
}

constexpr int a_expgap(int e) { constexpr int t[6] = {0, 0, 1, 2, 3, 4}; return 5 * (e / 6) + t[e % 6]; }
constexpr int a_cvtgap(int q) { constexpr int t[3] = {2, 3, 6}; return 5 * (q / 3) + t[q % 3]; }
constexpr int a_maxgap(int k) { constexpr int t[16] = {9, 12, 13, 14, 17, 18, 19, 22, 23, 24, 25, 26, 26, 27, 27, 28}; return t[k]; }
constexpr int a_dmaat(int G) { constexpr int t[8] = {13, 15, 17, 19, 21, 23, 25, 27}; for (int k = 0; k < 8; ++k) if (t[k] == G) return k; return -1; }

template <int X, int MODE> DI void a_step(AState& st, const AUni& un, const int u, const int dq_in) {
  constexpr bool FIRST = MODE == 2;
  constexpr int Xp = X ^ 1;
  bf16x8 kf[16], vf[16];
#pragma unroll
  for (int i = 0; i < 4; ++i) kf[i] = st.kf[i];
  float ex[32], mx = 0.f, rmax = 0.f;
  int dq = dq_in;
  const unsigned evmask = (1u << (un.n0 - 1)) | (1u << 16), evspec = 3u << (un.n0 - 1);
  const unsigned domask = MODE == 1 ? (evmask >> u) & 1u : 0u;
  unsigned special = MODE == 1 ? (evspec >> u) & 1u : 0u;
  constexpr unsigned KO = X * 32768u, KN = (X ^ 1) * 32768u, VO = (X ^ 1) * 32768u;
  const unsigned sok = un.sk + (unsigned)a_pair(un, u + 2) * 32768u, sov = un.sk + (unsigned)a_pair(un, u) * 32768u, slotoff = X * 32768u;
  static_for<32>([&](auto gc) {
    constexpr int G = decltype(gc)::value;
    if constexpr (G == 12) {
      asm volatile("s_waitcnt vmcnt(0) lgkmcnt(0)\n\ts_barrier" ::: "memory");
      SBAR();
    }
    if constexpr (G < 8)       st.S[X][0] = mfma32(kf[G], st.Q[G], G == 0 ? st.negm : st.S[X][0]);
    else if constexpr (G < 16) st.S[X][1] = mfma32(kf[G], st.Q[G - 8], G == 8 ? st.negm : st.S[X][1]);
    else if constexpr (!FIRST) st.O[G & 3] = mfma32(vf[G - 16], pfrag(st.P[(G - 16) >> 2]), st.O[G & 3]);
    if constexpr (G < 4) kf[G + 4] = a_rd(st.kaddr + KO + (G + 4) * 1024);
    if constexpr (G >= 4 && G < 8) { kf[2 * G] = a_rd(st.kaddr + KO + (2 * G) * 1024); }
    if constexpr (G >= 4 && G < 8) { kf[2 * G + 1] = a_rd(st.kaddr + KO + (2 * G + 1) * 1024); }
    if constexpr (!FIRST && G >= 12 && G < 28) vf[G - 12] = a_rd(st.vaddr + VO + (G - 12) * 1024);
    if constexpr (G >= 28) st.kf[G - 28] = a_rd(st.kaddr + KN + (G - 28) * 1024);
    if constexpr (G == 12) SET_M0(un.m0k + slotoff);
    if constexpr (G == 20) SET_M0(un.m0v + slotoff);
    if constexpr (a_dmaat(G) >= 0) {
      constexpr int K = a_dmaat(G);
      if constexpr (K < 4) DMA16_OFF(un.vlin, un.rk, sok, K * 1024); else DMA16_OFF(un.vlin, un.rv, sov, (K - 4) * 1024);
    }
    if constexpr (!FIRST) {
      static_for<32>([&](auto ec) {
        constexpr int e = decltype(ec)::value;
        if constexpr (a_expgap(e) + 1 == G) { if constexpr (e & 1) { st.l1 += ex[e]; PINV(st.l1); } else { st.l0 += ex[e]; PINV(st.l0); } }
      });
      static_for<16>([&](auto qc) {
        constexpr int q = decltype(qc)::value;
        if constexpr (a_cvtgap(q) == G) { unsigned pk = cvtpk(ex[2 * q], ex[2 * q + 1]); PINV(pk); st.P[q >> 2][q & 3] = pk; }
      });
      static_for<32>([&](auto ec) {
        constexpr int e = decltype(ec)::value;
        if constexpr (a_expgap(e) == G) { ex[e] = AEXP(st.S[Xp][e >> 4][e & 15]); PINV(ex[e]); }
      });
    }
    if constexpr (MODE == 1 && G == 9) { if (__builtin_expect(domask != 0u, 0)) { asm volatile("s_nop 7"); PINV(dq); mask16(st.S[X][0], dq, 0); } }
    if constexpr (MODE == 1 && G == 18) {
      if (__builtin_expect(domask != 0u, 0)) {
        asm volatile("s_nop 7"); PINV(dq); mask16(st.S[X][1], dq, 32);
        if (u == un.n0 - 1) {
          const unsigned qo = (un.h * 64u + (unsigned)(16 - un.n0) * 4u + (un.w & 3u)) * 8192u;
#define QLD(i) asm volatile("buffer_load_dwordx4 %0, %1, %2, %3 offen offset:%4" : "+v"(st.Q[i]) : "v"(un.vlin), "s"(un.rq), "s"(qo + ((i) >> 2) * 4096u), "i"(((i) & 3) * 1024) : "memory")
          QLD(0); QLD(1); QLD(2); QLD(3); QLD(4); QLD(5); QLD(6); QLD(7);
#undef QLD
        }
      }
    }
    static_for<16>([&](auto kc) {
      constexpr int k = decltype(kc)::value;
      if constexpr (a_maxgap(k) == G) {
        if constexpr (k == 0) mx = max3f(st.S[X][0][0], st.S[X][0][1], st.S[X][0][2]);
        else if constexpr (k == 15) mx = __builtin_fmaxf(mx, st.S[X][1][15]);
        else mx = max3f(mx, st.S[X][(2 * k + 1) >> 4][(2 * k + 1) & 15], st.S[X][(2 * k + 2) >> 4][(2 * k + 2) & 15]);
        PINV(mx);
      }
    });
    if constexpr (G == 29) {
      auto pr = __builtin_amdgcn_permlane32_swap(__float_as_uint(mx), __float_as_uint(mx), false, false);
      rmax = __builtin_fmaxf(__uint_as_float(pr[0]), __uint_as_float(pr[1]));
      PINV(rmax);
    }
    if constexpr (G == 30 && !FIRST) {
      const unsigned long long bal = __builtin_amdgcn_ballot_w64(rmax > THR);
      special |= (unsigned)bal | (unsigned)(bal >> 32);
    }
    SBAR();
  });
  if constexpr (FIRST) {
    const float dl = __builtin_fmaxf(rmax, -1e30f);
    st.mhat = dl;
    sub16(st.S[X][0], dl); sub16(st.S[X][1], dl); bcast16(st.negm, -dl);
  } else {
    const bool first = u == un.n0, seam = u == un.n0 - 1;
    if (__builtin_expect(special != 0u, 0)) {
      asm volatile("s_nop 7\n\ts_nop 7");
      if (first)
        a_epilogue(st, un, st.mA, st.l0 + st.l1, un.n0 - 1, un.ldsb + 65536u + VO, un.ldsb + 131072u);
      const float dl = __builtin_fmaxf(rmax, first ? -1e30f : 0.f);
      st.mhat += dl;
      const float f = first ? 0.f : __builtin_amdgcn_exp2f(-dl);
      st.l0 *= f; st.l1 *= f;
#pragma unroll
      for (int d = 0; d < 4; ++d) mul16(st.O[d], f);
      sub16(st.S[X][0], dl); sub16(st.S[X][1], dl); bcast16(st.negm, -st.mhat);
    if (seam) {
      st.mA = st.mhat; st.mhat = 0.f; bcast16(st.negm, 0.f);
      asm volatile("s_waitcnt vmcnt(5)" : "+v"(st.Q[0]), "+v"(st.Q[1]), "+v"(st.Q[2]), "+v"(st.Q[3]), "+v"(st.Q[4]), "+v"(st.Q[5]), "+v"(st.Q[6]), "+v"(st.Q[7]) :: "memory");
    }
    }
  }
  SBAR();
}

__global__ __launch_bounds__(512, 2) void attn_kernel(const u16* __restrict__ Qg, const u16* __restrict__ Kg, const u16* __restrict__ Vg,
                                                       u16* __restrict__ ctx, const ConvArgs ca) {
  extern __shared__ __attribute__((aligned(16))) char lds[];
  const unsigned tid = threadIdx.x, lane = tid & 63u, w = rfl(tid >> 6), r = lane & 31u, hi = lane >> 5;
  const unsigned bid = blockIdx.x, idx = bid >> 3, h = (bid & 7u) * 4u + (idx >> 3), p = idx & 7u;
  const unsigned g = w >> 2, rw = w & 3u;
  const unsigned ldsb = (unsigned)(size_t)(LAS char*)lds;
  AState st;
  AUni un;
  un.vlin = lane * 16u;
  un.m0k = ldsb + w * 4096u; un.m0v = ldsb + 65536u + w * 4096u;
  un.sk = h * (64u * 8192u) + w * 4096u;
  un.ldsb = ldsb; un.w = w; un.g = g; un.h = h; un.n0 = 16 - (int)p; un.ctx = ctx;
  un.rk = __builtin_amdgcn_make_buffer_rsrc((void*)Kg, (short)0, NH * S * HD * 2, SRD3);
  un.rv = __builtin_amdgcn_make_buffer_rsrc((void*)Vg, (short)0, NH * S * HD * 2, SRD3);
  un.rq = __builtin_amdgcn_make_buffer_rsrc((void*)Qg, (short)0, NH * S * HD * 2, SRD3);
  st.kaddr = ldsb + g * 16384u + lane * 16u;
  st.vaddr = ldsb + 65536u + g * 16384u + lane * 16u;
  PINV(st.kaddr); PINV(st.vaddr);
  STAMPR(1, 38); STAMP(1, 0);
  a_dma_k(un, 0, 0);
  {
    const u16* qp = Qg + ((size_t)(h * 64u + (15u - p) * 4u + rw) * 8u * 64u + lane) * 8u;
#pragma unroll
    for (int d0 = 0; d0 < 8; ++d0) st.Q[d0] = *(const bf16x8*)(qp + d0 * 512);
  }
  a_dma_k(un, 1, 1);
  STAMP(1, 21);
  {
    ConvRegs cr;
    conv_issue(cr, ca, bid, tid);
    SBAR();
    STAMP(1, 22);
    conv_run(cr, ca, bid, tid, ldsb + 65536u);
  }
  STAMP(1, 23);
#pragma unroll
  for (int d = 0; d < 4; ++d) st.O[d] = f32x16{};
  st.negm = f32x16{}; st.l0 = 0.f; st.l1 = 0.f; st.mhat = 0.f; st.mA = 0.f;
  asm volatile("s_waitcnt vmcnt(2)\n\ts_barrier" ::: "memory");
  SBAR();
#pragma unroll
  for (int i = 0; i < 4; ++i) st.kf[i] = lds_rd128(st.kaddr + i * 1024);
  const int dq = (int)(rw * 32u + r) - (int)(g * 64u) - 4 * (int)hi;
  STAMP(1, 1);
  a_step<0, 2>(st, un, 0, dq);
  STAMP(1, 2);
  for (int u = 1; u <= 16; u += 2) {
    a_step<1, 1>(st, un, u, dq);
    STEPSTAMP(1, 2 + u);
    a_step<0, 1>(st, un, u + 1, dq);
    STEPSTAMP(1, 3 + u);
  }
  STAMP(1, 18);
  {
    float ex[32];
#pragma unroll
    for (int e = 0; e < 32; ++e) { ex[e] = AEXP(st.S[0][e >> 4][e & 15]); if (e & 1) st.l1 += ex[e]; else st.l0 += ex[e]; }
#pragma unroll
    for (int q = 0; q < 16; ++q) st.P[q >> 2][q & 3] = cvtpk(ex[2 * q], ex[2 * q + 1]);
  }
  SBAR();
  asm volatile("s_waitcnt vmcnt(0) lgkmcnt(0)\n\ts_barrier" ::: "memory");
  SBAR();
#pragma unroll
  for (int j = 0; j < 16; ++j) {
    const bf16x8 vf = lds_rd128(st.vaddr + j * 1024);
    st.O[j & 3] = mfma32(vf, pfrag(st.P[j >> 2]), st.O[j & 3]);
  }
  STAMP(1, 19);
  a_epilogue(st, un, st.mhat, st.l0 + st.l1, (int)p, ldsb, ldsb + 131072u);
  STAMP(1, 20); STAMPR(1, 39);
}

typedef __attribute__((address_space(1))) unsigned gu32;
struct OpArgs { const u16 *ctx, *wf, *gbuf, *gaT; u16* out; float* part; unsigned* cnt; };
constexpr int OP_SLOT = 36864;
struct OpUni { unsigned m0[3], so[3]; unsigned isb[3]; __amdgpu_buffer_rsrc_t ra, rb; unsigned vo; };
template <int SLOT> DI void op_dma(const OpUni& u, const __amdgpu_buffer_rsrc_t (&rs)[3], int st, unsigned w) {
#pragma unroll
  for (int q = 0; q < 3; ++q) {
    if (q < 2 || w < 2u) {
      SET_M0(u.m0[q] + SLOT * OP_SLOT);
      DMA16_OFF(u.vo, rs[q], u.so[q] + (unsigned)st * 2048u, 0); DMA16_OFF(u.vo, rs[q], u.so[q] + (unsigned)st * 2048u, 1024);
    }
  }
}
__global__ __launch_bounds__(512, 2) void oproj_kernel(const OpArgs a) {
  extern __shared__ __attribute__((aligned(16))) char lds[];
  const unsigned tid = threadIdx.x, lane = tid & 63u, w = rfl(tid >> 6);
  const unsigned bid = blockIdx.x, xcd = bid & 7u, idx = bid >> 3;
  const unsigned kq = xcd & 3u, mt = (xcd >> 2) * 8u + (idx & 7u), nt = idx >> 3, tile = mt * 4u + nt;
  const unsigned wm = w >> 1, wn = w & 1u;
  const unsigned ldsb = (unsigned)(size_t)(LAS char*)lds;
  STAMPR(2, 38); STAMP(2, 0);
  OpUni u;
  __amdgpu_buffer_rsrc_t rs[3];
  u.vo = lane * 16u;
  {
    const __amdgpu_buffer_rsrc_t ra = __builtin_amdgcn_make_buffer_rsrc((char*)a.ctx + ((size_t)(mt * 8u) * 128u + kq * 32u) * 1024u, (short)0, 8 * 128 * 1024, SRD3);
    const __amdgpu_buffer_rsrc_t rb = __builtin_amdgcn_make_buffer_rsrc((char*)a.wf + ((size_t)(nt * 10u) * 128u + kq * 32u) * 1024u, (short)0, 10 * 128 * 1024, SRD3);
#pragma unroll
    for (int q = 0; q < 3; ++q) {
      const unsigned un = w + 8u * q;
      u.m0[q] = ldsb + un * 2048u;
      u.so[q] = (un < 8u ? un : un - 8u) * 131072u;
      rs[q] = (q == 0) ? ra : rb;
    }
  }
  op_dma<0>(u, rs, 0, w); op_dma<1>(u, rs, 1, w); op_dma<2>(u, rs, 2, w); op_dma<3>(u, rs, 3, w);
  u16 ga[3][4], omg[3];
  f32x4 acc[2][5];
#pragma unroll
  for (int i = 0; i < 2; ++i)
#pragma unroll
    for (int j = 0; j < 5; ++j) acc[i][j] = f32x4{};
  unsigned fa0 = ldsb + (2u * wm) * 2048u + lane * 16u, fb0 = ldsb + (8u + 5u * wn) * 2048u + lane * 16u;
  PINV(fa0); PINV(fb0);
  unsigned fa1 = fa0 + 2u * OP_SLOT, fb1 = fb0 + 2u * OP_SLOT;
  PINV(fa1); PINV(fb1);
  asm volatile("s_waitcnt vmcnt(12)\n\ts_barrier" ::: "memory");
  SBAR();
  STAMP(2, 1);
  bf16x8 ca[2][2], cb[2][5];
  auto rdf = [&](auto slotc, auto ksc, int buf) {
    constexpr int SL = decltype(slotc)::value, KS = decltype(ksc)::value;
    const unsigned pa = SL < 2 ? fa0 : fa1, pb = SL < 2 ? fb0 : fb1;
    constexpr int so = (SL & 1) * OP_SLOT + KS * 1024;
#pragma unroll
    for (int i = 0; i < 2; ++i) ca[buf][i] = lds_rd128(pa + so + i * 2048);
#pragma unroll
    for (int j = 0; j < 5; ++j) cb[buf][j] = lds_rd128(pb + so + j * 2048);
  };
  auto mm = [&](int buf) {
#pragma unroll
    for (int i = 0; i < 2; ++i)
#pragma unroll
      for (int j = 0; j < 5; ++j) acc[i][j] = mfma16(cb[buf][j], ca[buf][i], acc[i][j]);
  };
  rdf(std::integral_constant<int, 0>{}, std::integral_constant<int, 0>{}, 0);
  static_for<16>([&](auto sc) {
    constexpr int st = decltype(sc)::value, SL = st & 3;
    rdf(std::integral_constant<int, SL>{}, std::integral_constant<int, 1>{}, 1);
    SBAR();
    mm(0);
    SBAR();
    if constexpr (st < 15) {
      if constexpr (st < 13)       asm volatile("s_waitcnt vmcnt(8) lgkmcnt(0)\n\ts_barrier" ::: "memory");
      else if constexpr (st == 13) asm volatile("s_waitcnt vmcnt(4) lgkmcnt(0)\n\ts_barrier" ::: "memory");
      else                         asm volatile("s_waitcnt vmcnt(0) lgkmcnt(0)\n\ts_barrier" ::: "memory");
      SBAR();
      rdf(std::integral_constant<int, (st + 1) & 3>{}, std::integral_constant<int, 0>{}, 0);
      if constexpr (st + 4 < 16) op_dma<SL>(u, rs, st + 4, w);
      if constexpr (st == 14) {
#pragma unroll
        for (int k = 0; k < 3; ++k) {
          unsigned td = tid; PINV(td);
          const unsigned it = td + 512u * k, tl = (it < 1280u ? it : 0u) >> 6, ln = it & 63u, i = tl / 10u, j = tl - i * 10u;
          const unsigned t = mt * 128u + kq * 32u + i * 16u + (ln & 15u), c = nt * 160u + j * 16u + 4u * (ln >> 4);
          omg[k] = a.gbuf[S + t];
#pragma unroll
          for (int e = 0; e < 4; ++e) ga[k][e] = a.gaT[(size_t)(c + e) * S + t];
        }
      }
      SBAR();
    }
    mm(1);
    SBAR();
  });
  STAMP(2, 2);
  const __amdgpu_buffer_rsrc_t rp = __builtin_amdgcn_make_buffer_rsrc((char*)a.part + (size_t)tile * (16u * 20480u), (short)0, 16 * 20480, SRD3);
  asm volatile("s_barrier" ::: "memory");
  if (wm != kq) {
#pragma unroll
    for (int i = 0; i < 2; ++i)
#pragma unroll
      for (int j = 0; j < 5; ++j)
        bst128<16>(__builtin_bit_cast(u32x4, acc[i][j]), rp, lane * 16u, (wm * 4u + kq) * 20480u + (i * 10u + wn * 5u + j) * 1024u);
  } else {
#pragma unroll
    for (int i = 0; i < 2; ++i)
#pragma unroll
      for (int j = 0; j < 5; ++j) *(LAS f32x4*)(size_t)(ldsb + (i * 10u + wn * 5u + j) * 1024u + lane * 16u) = acc[i][j];
  }
  asm volatile("s_waitcnt vmcnt(0) lgkmcnt(0)\n\ts_barrier" ::: "memory");
  STAMP(2, 3);
  if (tid < 4u && tid != kq) __hip_atomic_fetch_add((gu32*)(a.cnt + (tile * 4u + tid) * 64u), 1u, __ATOMIC_RELAXED, __HIP_MEMORY_SCOPE_AGENT);
  if (w == 0u) {
    gu32* cp = (gu32*)(a.cnt + (tile * 4u + kq) * 64u);
    unsigned spins = 0;
    while (rfl(__hip_atomic_load(cp, __ATOMIC_RELAXED, __HIP_MEMORY_SCOPE_AGENT)) < 3u && ++spins < (1u << 22)) __builtin_amdgcn_s_sleep(2);
  }
  asm volatile("s_barrier" ::: "memory");
  STAMP(2, 4);
#pragma unroll
  for (int k = 0; k < 3; ++k) {
    const unsigned it = tid + 512u * k;
    if (it < 1280u) {
      const unsigned tl = it >> 6, ln = it & 63u, i = tl / 10u, j = tl - i * 10u;
      f32x4 b = *(const LAS f32x4*)(size_t)(ldsb + it * 16u);
#pragma unroll
      for (int q = 1; q < 4; ++q)
        b += __builtin_bit_cast(f32x4, __builtin_amdgcn_raw_buffer_load_b128(rp, it * 16u, (kq * 4u + ((kq + q) & 3u)) * 20480u, 16));
      const unsigned t = mt * 128u + kq * 32u + i * 16u + (ln & 15u), c = nt * 160u + j * 16u + 4u * (ln >> 4);
      float o[4];
#pragma unroll
      for (int e = 0; e < 4; ++e) o[e] = bf2f(ga[k][e]) + rbf(bf2f(omg[k]) * rbf(b[e]));
      *(u32x2*)(a.out + (size_t)t * DM + c) = u32x2{cvtpk(o[0], o[1]), cvtpk(o[2], o[3])};
    }
  }
  STAMP(2, 5); STAMPR(2, 39);
}

extern "C" void kernel_launch(void* const* d_in, const int* in_sizes, int n_in, void* d_out, int out_size, void* d_ws, size_t ws_size,
                              hipStream_t stream) {
  const u16* A_q = (const u16*)d_in[0];
  const u16* A_k = (const u16*)d_in[1];
  const u16* A_v = (const u16*)d_in[2];
  const u16* A_shk = (const u16*)d_in[3];
  const u16* A_ssmk = (const u16*)d_in[4];
  const u16* A_ssmD = (const u16*)d_in[5];
  const u16* B_ckv = (const u16*)d_in[7];
  const u16* B_wq = (const u16*)d_in[8];
  const u16* B_wuk = (const u16*)d_in[9];
  const u16* B_wuv = (const u16*)d_in[10];
  const u16* B_wo = (const u16*)d_in[11];
  const u16* gate_w = (const u16*)d_in[12];
  char* ws = (char*)d_ws;
  u16* Kg = (u16*)(ws + WS_K); u16* Vg = (u16*)(ws + WS_V); u16* Qg = (u16*)(ws + WS_Q); u16* ctx = (u16*)(ws + WS_CTX);
  u16* wf = (u16*)(ws + WS_WO); u16* uT = (u16*)(ws + WS_UT); u16* xT = (u16*)(ws + WS_XT); u16* gaT = (u16*)(ws + WS_GA);
  u16* gbuf = (u16*)(ws + WS_G);
  (void)hipFuncSetAttribute((const void*)proj_kernel, hipFuncAttributeMaxDynamicSharedMemorySize, 163840);
  (void)hipFuncSetAttribute((const void*)attn_kernel, hipFuncAttributeMaxDynamicSharedMemorySize, 163840);
  (void)hipFuncSetAttribute((const void*)oproj_kernel, hipFuncAttributeMaxDynamicSharedMemorySize, 4 * OP_SLOT);
  unsigned* cnt = (unsigned*)(ws + WS_CNT);
  SideArgs sa; sa.x = A_q; sa.k = A_k; sa.v = A_v; sa.shk = A_shk; sa.gw = gate_w; sa.wo = B_wo; sa.uT = uT; sa.xT = xT; sa.wf = wf; sa.gbuf = gbuf;
  ConvArgs ca; ca.hk = A_ssmk; ca.sD = A_ssmD; ca.uT = uT; ca.xT = xT; ca.gbuf = gbuf; ca.gaT = gaT;
  OpArgs oa; oa.ctx = ctx; oa.wf = wf; oa.gbuf = gbuf; oa.gaT = gaT; oa.out = (u16*)d_out; oa.part = (float*)(ws + WS_PART); oa.cnt = cnt;
  proj_kernel<<<256, 512, 163840, stream>>>(A_q, B_ckv, B_wq, B_wuk, B_wuv, Qg, Kg, Vg, cnt, sa, A_ssmk);
  attn_kernel<<<256, 512, 163840, stream>>>(Qg, Kg, Vg, ctx, ca);
  oproj_kernel<<<256, 512, 4 * OP_SLOT, stream>>>(oa);
}
```
